```python
import torch

def _softmax(x, axis=-1):
    return torch.softmax(x, dim=axis)
def _gelu(x):
    return torch.nn.functional.gelu(x, approximate="tanh")
def _silu(x):
    return torch.nn.functional.silu(x)
def _sigmoid(x):
    return torch.sigmoid(x)
def _layernorm(x, axis=-1, eps=1e-5):
    m = x.mean(dim=axis, keepdim=True)
    v = x.var(dim=axis, keepdim=True, unbiased=False)
    return (x - m) / torch.sqrt(v + eps)
def _scatter_add(tgt, idx, src, axis=0):
    return tgt.clone().index_add_(axis, idx, src)
def _segment_sum(src, seg, n):
    out = torch.zeros((n,) + src.shape[1:], dtype=src.dtype)
    return out.index_add_(0, seg, src)
def _pad(x, pw):
    flat = tuple(v for pair in reversed(pw) for v in pair)
    return torch.nn.functional.pad(x, flat)

kernel_name = 'standalone__gqa_block_cached__near_ridge'

def setup_inputs():

    _w = (lambda a: (lambda t: t.to(torch.bfloat16) if t.is_floating_point() else t)(torch.as_tensor(a)))
    B, Sq, D = 16, 64, 576
    Hq, Hkv, Sk, Dh = 32, 16, 1024, 128
    assert Hq % Hkv == 0, (Hq, Hkv)
    return {
        "x": _w((lambda *s: torch.randn(*s, dtype=torch.bfloat16) * 0.8)(B, Sq, D)),
        "k_cache": _w((lambda *s: torch.randn(*s, dtype=torch.bfloat16) * 0.8)(B, Hkv, Sk, Dh)),
        "v_cache": _w((lambda *s: torch.randn(*s, dtype=torch.bfloat16) * 0.05)(B, Hkv, Sk, Dh)),
        "wq": _w((lambda *s: torch.randn(*s, dtype=torch.bfloat16) * 0.8)(D, Hq * Dh) * (D ** -0.5)),
        "wo": _w((lambda *s: torch.randn(*s, dtype=torch.bfloat16) * 0.05)(Hq * Dh, D) * ((Hq * Dh) ** -0.5)),
    }

def reference(x, k_cache, v_cache, wq, wo):
    B, Sq, D = x.shape
    Hq, Dh = 32, 128
    Hkv = k_cache.shape[1]
    rep = Hq // Hkv
    q = (lambda x, *d: x.permute(*d))((x @ wq).reshape(B, Sq, Hq, Dh), 0, 2, 1, 3)
    k = (lambda x, n, axis: torch.repeat_interleave(x, n, dim=axis))(k_cache, rep, axis=1)
    v = (lambda x, n, axis: torch.repeat_interleave(x, n, dim=axis))(v_cache, rep, axis=1)
    scores = torch.einsum("bhqd,bhkd->bhqk", q, k) * (Dh ** -0.5)
    p = _softmax(scores, axis=-1)
    o = torch.einsum("bhqk,bhkd->bhqd", p, v)
    return (lambda x, *d: x.permute(*d))(o, 0, 2, 1, 3).reshape(B, Sq, Hq * Dh) @ wo
```

```cpp
#include <hip/hip_runtime.h>
#include <hip/hip_bf16.h>
#include <stdint.h>

typedef __attribute__((ext_vector_type(8))) short bf16x8;
typedef __attribute__((ext_vector_type(4))) short s16x4;
typedef __attribute__((ext_vector_type(16))) float f32x16;
typedef __attribute__((ext_vector_type(4))) float f32x4;
typedef __attribute__((ext_vector_type(2))) float f32x2;
typedef __attribute__((ext_vector_type(4))) unsigned u32x4;
typedef __attribute__((ext_vector_type(2))) unsigned u32x2;
typedef __attribute__((ext_vector_type(2))) __bf16 bf16x2_t;
typedef unsigned short u16;
typedef __attribute__((address_space(3))) s16x4* lds_s16x4_p;

#define DI __device__ __forceinline__

constexpr int NB = 16, SQ = 64, DM = 576, HQ = 32, HKV = 16, SK = 1024, DH = 128;
constexpr int MROWS = NB * SQ;
constexpr int QW = HQ * DH;

DI unsigned cvtpk(float lo, float hi) {
  f32x2 v = {lo, hi};
  bf16x2_t b = __builtin_convertvector(v, bf16x2_t);
  return __builtin_bit_cast(unsigned, b);
}
DI s16x4 tr_read(const char* p) {
  return __builtin_amdgcn_ds_read_tr16_b64_v4i16((lds_s16x4_p)(p));
}
DI f32x16 mfma32(bf16x8 a, bf16x8 b, f32x16 c) { return __builtin_amdgcn_mfma_f32_32x32x16_bf16(a, b, c, 0, 0, 0); }

__global__ __launch_bounds__(256) void gemm_simple(const u16* __restrict__ A, const u16* __restrict__ Bm, u16* __restrict__ C,
                                                   int N, int K) {
  __shared__ __attribute__((aligned(16))) char lds[64 * 80 + 32 * 192];
  char* As = lds;
  char* Bs = lds + 64 * 80;
  const int tid = threadIdx.x, wid = tid >> 6, lane = tid & 63, r = lane & 31, h = lane >> 5;
  const int m0 = blockIdx.y * 64, n0 = blockIdx.x * 64;
  const int wm = wid >> 1, wn = wid & 1;
  f32x16 acc = {};
  const int arow = tid >> 2, ach = tid & 3;
  const int brow = tid >> 3, bch = tid & 7;
  const int blk = (lane >> 4) & 1, q4 = (lane & 15) >> 2, p4 = lane & 3;
  for (int k0 = 0; k0 < K; k0 += 32) {
    bf16x8 av = *(const bf16x8*)(A + (size_t)(m0 + arow) * K + k0 + ach * 8);
    bf16x8 bv = *(const bf16x8*)(Bm + (size_t)(k0 + brow) * N + n0 + bch * 8);
    __syncthreads();
    *(bf16x8*)(As + arow * 80 + ach * 16) = av;
    *(bf16x8*)(Bs + brow * 192 + bch * 16) = bv;
    __syncthreads();
#pragma unroll
    for (int s = 0; s < 2; ++s) {
      bf16x8 xa = *(const bf16x8*)(As + (wm * 32 + r) * 80 + s * 32 + h * 16);
      s16x4 lo = tr_read(Bs + (16 * s + 8 * h + q4) * 192 + (wn * 32 + 16 * blk + 4 * p4) * 2);
      s16x4 hi = tr_read(Bs + (16 * s + 8 * h + 4 + q4) * 192 + (wn * 32 + 16 * blk + 4 * p4) * 2);
      bf16x8 wb = __builtin_shufflevector(lo, hi, 0, 1, 2, 3, 4, 5, 6, 7);
      acc = mfma32(wb, xa, acc);
    }
  }
  u16* crow = C + (size_t)(m0 + wm * 32 + r) * N + n0 + wn * 32;
#pragma unroll
  for (int a = 0; a < 4; ++a) {
    u32x2 w = {cvtpk(acc[4 * a], acc[4 * a + 1]), cvtpk(acc[4 * a + 2], acc[4 * a + 3])};
    *(u32x2*)(crow + 8 * a + 4 * h) = w;
  }
}

constexpr int NS = 4;
constexpr int SLOT = 32768;
constexpr int ATT_LDS = NS * SLOT;
constexpr float SC = 0.08838834764831845f * 1.4426950408889634f;

DI void dma16(unsigned ldsaddr, unsigned voff, const void* sbase) {
  unsigned keep;
  asm volatile("s_mov_b32 %0, m0\n\ts_mov_b32 m0, %1\n\ts_nop 0\n\tglobal_load_lds_dwordx4 %2, %3\n\ts_mov_b32 m0, %0"
               : "=&s"(keep) : "s"(ldsaddr), "v"(voff), "s"(sbase) : "memory");
}

struct AttnState {
  f32x16 o[4];
  float m, l;
};

template <int N> DI void wait_vm() { asm volatile("s_waitcnt vmcnt(%0)" ::"i"(N) : "memory"); }

struct AttnCtx {
  const char* kbase;
  const char* vbase;
  unsigned koff[2], voff[2];
  unsigned ldsw;
  unsigned kaddr[8];
  unsigned vaddr[8];
};

DI void issue_tile(const AttnCtx& c, int t, int slot) {
  const char* kb = c.kbase + (size_t)t * 16384;
  const char* vb = c.vbase + (size_t)t * 16384;
  unsigned l0 = slot * SLOT + c.ldsw;
  dma16(l0, c.koff[0], kb);
  dma16(l0 + 1024, c.koff[1], kb);
  dma16(l0 + 16384, c.voff[0], vb);
  dma16(l0 + 16384 + 1024, c.voff[1], vb);
}

template <int SLOTI> DI void compute_tile(const AttnCtx& c, AttnState& st, const bf16x8* qf, const char* lds) {
  const char* Ks = lds + SLOTI * SLOT;
  const char* Vs = Ks + 16384;
  f32x16 s = {};
#pragma unroll
  for (int i = 0; i < 8; ++i) {
    bf16x8 a = *(const bf16x8*)(Ks + c.kaddr[i]);
    s = mfma32(a, qf[i], s);
  }
  float pm = s[0];
#pragma unroll
  for (int i = 1; i < 16; ++i) pm = fmaxf(pm, s[i]);
  pm = fmaxf(pm, __shfl_xor(pm, 32));
  const float mnew = fmaxf(st.m, pm);
  const float alpha = __builtin_amdgcn_exp2f((st.m - mnew) * SC);
  const float nm = -mnew * SC;
  float ps = 0.f;
#pragma unroll
  for (int i = 0; i < 16; ++i) { s[i] = __builtin_amdgcn_exp2f(fmaf(s[i], SC, nm)); ps += s[i]; }
  st.l = st.l * alpha + ps;
  st.m = mnew;
  if (__any(alpha != 1.0f)) {
#pragma unroll
    for (int d = 0; d < 4; ++d)
#pragma unroll
      for (int i = 0; i < 16; ++i) st.o[d][i] *= alpha;
  }
  bf16x8 pf[2];
#pragma unroll
  for (int k = 0; k < 2; ++k) {
    u32x4 w = {cvtpk(s[8 * k], s[8 * k + 1]), cvtpk(s[8 * k + 2], s[8 * k + 3]), cvtpk(s[8 * k + 4], s[8 * k + 5]), cvtpk(s[8 * k + 6], s[8 * k + 7])};
    pf[k] = __builtin_bit_cast(bf16x8, w);
  }
#pragma unroll
  for (int d = 0; d < 4; ++d) {
#pragma unroll
    for (int k = 0; k < 2; ++k) {
      s16x4 lo = tr_read(Vs + c.vaddr[2 * d] + k * 4096);
      s16x4 hi = tr_read(Vs + c.vaddr[2 * d + 1] + k * 4096);
      bf16x8 vf = __builtin_shufflevector(lo, hi, 0, 1, 2, 3, 4, 5, 6, 7);
      st.o[d] = mfma32(vf, pf[k], st.o[d]);
    }
  }
}

template <int SLOTI, int WAITN, bool ISSUE> DI void step(const AttnCtx& c, AttnState& st, const bf16x8* qf, const char* lds, int t) {
  wait_vm<WAITN>();
  asm volatile("s_waitcnt lgkmcnt(0)" ::: "memory");
  __builtin_amdgcn_s_barrier();
  if (ISSUE) issue_tile(c, t + NS - 1, (SLOTI + NS - 1) % NS);
  compute_tile<SLOTI>(c, st, qf, lds);
}

__global__ __launch_bounds__(512, 2) void attn_kernel(const u16* __restrict__ q, const u16* __restrict__ kc, const u16* __restrict__ vc,
                                                      u16* __restrict__ og) {
  extern __shared__ __attribute__((aligned(16))) char lds[];
  const int bid = blockIdx.x;
  const int hkv = 2 * (bid & 7) + ((bid >> 3) & 1), b = bid >> 4;
  const int tid = threadIdx.x, lane = tid & 63, r = lane & 31, h = lane >> 5;
  const int wid = __builtin_amdgcn_readfirstlane(tid >> 6);
  const int kg = wid >> 2, g = (wid >> 1) & 1, pb = wid & 1;
  const int head = hkv * 2 + g;

  AttnCtx c;
  c.kbase = (const char*)(kc + (size_t)(b * HKV + hkv) * SK * DH);
  c.vbase = (const char*)(vc + (size_t)(b * HKV + hkv) * SK * DH);
  c.ldsw = wid * 8 * 256;
#pragma unroll
  for (int i = 0; i < 2; ++i) {
    const int row = 8 * wid + 4 * i + (lane >> 4), pos = lane & 15;
    c.koff[i] = row * 256 + ((pos ^ (row & 15)) << 4);
    c.voff[i] = row * 256 + ((pos ^ (((row & 3) << 2) | ((row >> 2) & 3))) << 4);
  }
  {
    const int krow = 32 * kg + r;
#pragma unroll
    for (int i = 0; i < 8; ++i) c.kaddr[i] = krow * 256 + (((2 * i + h) ^ (r & 15)) << 4);
    const int blk = (lane >> 4) & 1, q4 = (lane & 15) >> 2, p4 = lane & 3;
#pragma unroll
    for (int d = 0; d < 4; ++d)
#pragma unroll
      for (int t = 0; t < 2; ++t) {
        const int row = 32 * kg + 8 * t + 4 * h + q4;
        const int ch = 4 * d + 2 * blk + (p4 >> 1);
        const int sw = ((row & 3) << 2) | ((row >> 2) & 3);
        c.vaddr[2 * d + t] = row * 256 + ((ch ^ sw) << 4) + 8 * (p4 & 1);
      }
  }
#pragma unroll
  for (int t = 0; t < NS - 1; ++t) issue_tile(c, t, t);

  bf16x8 qf[8];
  {
    const u16* qrow = q + (size_t)(b * SQ + 32 * pb + r) * QW + head * DH + 8 * h;
#pragma unroll
    for (int i = 0; i < 8; ++i) qf[i] = *(const bf16x8*)(qrow + 16 * i);
  }
  asm volatile("s_waitcnt vmcnt(0)" ::: "memory");
#pragma unroll
  for (int i = 0; i < 8; ++i) asm volatile("" : "+v"(qf[i]));

  AttnState st;
#pragma unroll
  for (int d = 0; d < 4; ++d) st.o[d] = f32x16{};
  st.m = -1e30f; st.l = 0.f;

  for (int t0 = 0; t0 < 12; t0 += 4) {
    step<0, 8, true>(c, st, qf, lds, t0);
    step<1, 8, true>(c, st, qf, lds, t0 + 1);
    step<2, 8, true>(c, st, qf, lds, t0 + 2);
    step<3, 8, true>(c, st, qf, lds, t0 + 3);
  }
  step<0, 8, true>(c, st, qf, lds, 12);
  step<1, 8, false>(c, st, qf, lds, 13);
  step<2, 4, false>(c, st, qf, lds, 14);
  step<3, 0, false>(c, st, qf, lds, 15);

  float lt = st.l + __shfl_xor(st.l, 32);
  __syncthreads();
  char* xw = lds + wid * 8704;
  char* xp = lds + (wid ^ 4) * 8704;
  {
#pragma unroll
    for (int dd = 0; dd < 2; ++dd) {
      const f32x16 v = kg ? st.o[dd] : st.o[2 + dd];
#pragma unroll
      for (int a = 0; a < 4; ++a) {
        f32x4 w = {v[4 * a], v[4 * a + 1], v[4 * a + 2], v[4 * a + 3]};
        *(f32x4*)(xw + ((dd * 4 + a) * 64 + lane) * 16) = w;
      }
    }
    f32x2 ml = {st.m, lt};
    *(f32x2*)(xw + 8192 + lane * 8) = ml;
  }
  __syncthreads();
  f32x2 pml = *(const f32x2*)(xp + 8192 + lane * 8);
  const float mm = fmaxf(st.m, pml[0]);
  const float fa = __builtin_amdgcn_exp2f((st.m - mm) * SC), fb = __builtin_amdgcn_exp2f((pml[0] - mm) * SC);
  const float inv = 1.0f / (lt * fa + pml[1] * fb);
  const float wa = fa * inv, wb = fb * inv;
  char* stg = lds + 8 * 8704 + wid * 4608;
#pragma unroll
  for (int dd = 0; dd < 2; ++dd) {
    const f32x16 own = kg ? st.o[2 + dd] : st.o[dd];
#pragma unroll
    for (int a = 0; a < 4; ++a) {
      f32x4 pv = *(const f32x4*)(xp + ((dd * 4 + a) * 64 + lane) * 16);
      float o0 = own[4 * a] * wa + pv[0] * wb, o1 = own[4 * a + 1] * wa + pv[1] * wb;
      float o2 = own[4 * a + 2] * wa + pv[2] * wb, o3 = own[4 * a + 3] * wa + pv[3] * wb;
      u32x2 w = {cvtpk(o0, o1), cvtpk(o2, o3)};
      *(u32x2*)(stg + r * 144 + (32 * dd + 8 * a + 4 * h) * 2) = w;
    }
  }
  asm volatile("s_waitcnt lgkmcnt(0)" ::: "memory");
  __builtin_amdgcn_wave_barrier();
  u16* obase = og + (size_t)(b * SQ + 32 * pb) * QW + head * DH + kg * 64;
#pragma unroll
  for (int i = 0; i < 4; ++i) {
    const int row = i * 8 + (lane >> 3), ch = lane & 7;
    u32x4 w = *(const u32x4*)(stg + row * 144 + ch * 16);
    *(u32x4*)(obase + (size_t)row * QW + ch * 8) = w;
  }
}

extern "C" void kernel_launch(void* const* d_in, const int* in_sizes, int n_in, void* d_out, int out_size, void* d_ws, size_t ws_size,
                              hipStream_t stream) {
  const u16* x = (const u16*)d_in[0];
  const u16* kc = (const u16*)d_in[1];
  const u16* vc = (const u16*)d_in[2];
  const u16* wq = (const u16*)d_in[3];
  const u16* wo = (const u16*)d_in[4];
  u16* out = (u16*)d_out;
  u16* qws = (u16*)d_ws;
  u16* ows = qws + (size_t)MROWS * QW;
  hipFuncSetAttribute((const void*)attn_kernel, hipFuncAttributeMaxDynamicSharedMemorySize, ATT_LDS);
  gemm_simple<<<dim3(QW / 64, MROWS / 64), 256, 0, stream>>>(x, wq, qws, QW, DM);
  attn_kernel<<<256, 512, ATT_LDS, stream>>>(qws, kc, vc, ows);
  gemm_simple<<<dim3(DM / 64, MROWS / 64), 256, 0, stream>>>(ows, wo, out, DM, QW);
}
```

```cpp
#include <hip/hip_runtime.h>
#include <hip/hip_bf16.h>
#include <stdint.h>

typedef __attribute__((ext_vector_type(8))) short bf16x8;
typedef __attribute__((ext_vector_type(4))) short s16x4;
typedef __attribute__((ext_vector_type(16))) float f32x16;
typedef __attribute__((ext_vector_type(4))) float f32x4;
typedef __attribute__((ext_vector_type(2))) float f32x2;
typedef __attribute__((ext_vector_type(4))) unsigned u32x4;
typedef __attribute__((ext_vector_type(2))) unsigned u32x2;
typedef __attribute__((ext_vector_type(2))) __bf16 bf16x2_t;
typedef unsigned short u16;
typedef __attribute__((address_space(3))) s16x4* lds_s16x4_p;

#define DI __device__ __forceinline__

constexpr int NB = 16, SQ = 64, DM = 576, HQ = 32, HKV = 16, SK = 1024, DH = 128;
constexpr int MROWS = NB * SQ;
constexpr int QW = HQ * DH;

DI unsigned cvtpk(float lo, float hi) {
  f32x2 v = {lo, hi};
  bf16x2_t b = __builtin_convertvector(v, bf16x2_t);
  return __builtin_bit_cast(unsigned, b);
}
DI s16x4 tr_read(const char* p) {
  return __builtin_amdgcn_ds_read_tr16_b64_v4i16((lds_s16x4_p)(p));
}
DI f32x16 mfma32(bf16x8 a, bf16x8 b, f32x16 c) { return __builtin_amdgcn_mfma_f32_32x32x16_bf16(a, b, c, 0, 0, 0); }

__global__ __launch_bounds__(256) void gemm_simple(const u16* __restrict__ A, const u16* __restrict__ Bm, u16* __restrict__ C,
                                                   int N, int K) {
  __shared__ __attribute__((aligned(16))) char lds[64 * 80 + 32 * 192];
  char* As = lds;
  char* Bs = lds + 64 * 80;
  const int tid = threadIdx.x, wid = tid >> 6, lane = tid & 63, r = lane & 31, h = lane >> 5;
  const int m0 = blockIdx.y * 64, n0 = blockIdx.x * 64;
  const int wm = wid >> 1, wn = wid & 1;
  f32x16 acc = {};
  const int arow = tid >> 2, ach = tid & 3;
  const int brow = tid >> 3, bch = tid & 7;
  const int blk = (lane >> 4) & 1, q4 = (lane & 15) >> 2, p4 = lane & 3;
  for (int k0 = 0; k0 < K; k0 += 32) {
    bf16x8 av = *(const bf16x8*)(A + (size_t)(m0 + arow) * K + k0 + ach * 8);
    bf16x8 bv = *(const bf16x8*)(Bm + (size_t)(k0 + brow) * N + n0 + bch * 8);
    __syncthreads();
    *(bf16x8*)(As + arow * 80 + ach * 16) = av;
    *(bf16x8*)(Bs + brow * 192 + bch * 16) = bv;
    __syncthreads();
#pragma unroll
    for (int s = 0; s < 2; ++s) {
      bf16x8 xa = *(const bf16x8*)(As + (wm * 32 + r) * 80 + s * 32 + h * 16);
      s16x4 lo = tr_read(Bs + (16 * s + 8 * h + q4) * 192 + (wn * 32 + 16 * blk + 4 * p4) * 2);
      s16x4 hi = tr_read(Bs + (16 * s + 8 * h + 4 + q4) * 192 + (wn * 32 + 16 * blk + 4 * p4) * 2);
      bf16x8 wb = __builtin_shufflevector(lo, hi, 0, 1, 2, 3, 4, 5, 6, 7);
      acc = mfma32(wb, xa, acc);
    }
  }
  u16* crow = C + (size_t)(m0 + wm * 32 + r) * N + n0 + wn * 32;
#pragma unroll
  for (int a = 0; a < 4; ++a) {
    u32x2 w = {cvtpk(acc[4 * a], acc[4 * a + 1]), cvtpk(acc[4 * a + 2], acc[4 * a + 3])};
    *(u32x2*)(crow + 8 * a + 4 * h) = w;
  }
}

constexpr int NS = 4;
constexpr int SLOT = 32768;
constexpr int ATT_LDS = NS * SLOT;
constexpr float SC = 0.08838834764831845f * 1.4426950408889634f;

DI void dma16(unsigned ldsaddr, unsigned voff, const void* sbase) {
  unsigned keep;
  asm volatile("s_mov_b32 %0, m0\n\ts_mov_b32 m0, %1\n\ts_nop 0\n\tglobal_load_lds_dwordx4 %2, %3 nt\n\ts_mov_b32 m0, %0"
               : "=&s"(keep) : "s"(ldsaddr), "v"(voff), "s"(sbase) : "memory");
}

struct AttnState {
  f32x16 o[4];
  f32x16 negm;
  float m, l;
};

template <int N> DI void wait_vm() { asm volatile("s_waitcnt vmcnt(%0)" ::"i"(N) : "memory"); }

struct AttnCtx {
  const char* kbase;
  const char* vbase;
  unsigned koff[2], voff[2];
  unsigned ldsw;
  unsigned kaddr[8];
  unsigned vaddr[8];
};


#define SBAR() __builtin_amdgcn_sched_barrier(0)
constexpr float THR = 8.0f;
template <int SL> DI void issue_k(const AttnCtx& c, int t) {
  const char* kb = c.kbase + (size_t)t * 16384;
  unsigned l0 = SL * 16384 + c.ldsw;
  dma16(l0, c.koff[0], kb);
  dma16(l0 + 1024, c.koff[1], kb);
}
template <int SL> DI void issue_v(const AttnCtx& c, int t) {
  const char* vb = c.vbase + (size_t)t * 16384;
  unsigned l0 = 65536 + SL * 16384 + c.ldsw;
  dma16(l0, c.voff[0], vb);
  dma16(l0 + 1024, c.voff[1], vb);
}
DI float rowmax16(const f32x16& s) {
  float pm = s[0];
#pragma unroll
  for (int i = 1; i < 16; ++i) pm = fmaxf(pm, s[i]);
  auto rr = __builtin_amdgcn_permlane32_swap(__float_as_uint(pm), __float_as_uint(pm), false, false);
  return fmaxf(__uint_as_float(rr[0]), __uint_as_float(rr[1]));
}
template <int SL, int WAITN, bool ISSUEK, bool ISSUEV, bool NEXT> DI void step(const AttnCtx& c, AttnState& st, f32x16& sc, const bf16x8* qf, const char* lds, int t) {
  wait_vm<WAITN>();
  asm volatile("s_waitcnt lgkmcnt(0)" ::: "memory");
  __builtin_amdgcn_s_barrier();
  SBAR();
  const char* Ks = lds + ((SL + 1) & 3) * 16384;
  const char* Vs = lds + SL * 16384;
  bf16x8 kf[8];
  if (NEXT) {
#pragma unroll
    for (int i = 0; i < 8; ++i) kf[i] = *(const bf16x8*)(Ks + c.kaddr[i]);
  }
  SBAR();
  if (ISSUEK) issue_k<SL>(c, t + 4);
  if (ISSUEV) issue_v<(SL + 3) & 3>(c, t + 3);
  SBAR();
  f32x16 sn;
  if (NEXT) {
    asm("v_mfma_f32_32x32x16_bf16 %0, %1, %2, %3" : "=&v"(sn) : "v"(kf[0]), "v"(qf[0]), "v"(st.negm));
#pragma unroll
    for (int i = 1; i < 8; ++i) sn = mfma32(kf[i], qf[i], sn);
  }
  s16x4 vlo[4][2], vhi[4][2];
#pragma unroll
  for (int k = 0; k < 2; ++k)
#pragma unroll
    for (int d = 0; d < 4; ++d) {
      vlo[d][k] = tr_read(Vs + c.vaddr[2 * d] + k * 4096);
      vhi[d][k] = tr_read(Vs + c.vaddr[2 * d + 1] + k * 4096);
    }
  float ps = 0.f;
#pragma unroll
  for (int i = 0; i < 16; ++i) { sc[i] = __builtin_amdgcn_exp2f(sc[i]); ps += sc[i]; }
  st.l += ps;
  bf16x8 pf[2];
#pragma unroll
  for (int k = 0; k < 2; ++k) {
    u32x4 w = {cvtpk(sc[8 * k], sc[8 * k + 1]), cvtpk(sc[8 * k + 2], sc[8 * k + 3]), cvtpk(sc[8 * k + 4], sc[8 * k + 5]), cvtpk(sc[8 * k + 6], sc[8 * k + 7])};
    pf[k] = __builtin_bit_cast(bf16x8, w);
  }
  if (NEXT) {
#pragma unroll
    for (int i = 0; i < 8; ++i) {
      __builtin_amdgcn_sched_group_barrier(0x8, 1, 0);
      __builtin_amdgcn_sched_group_barrier(0x100, 2, 0);
      __builtin_amdgcn_sched_group_barrier(0x2, 5, 0);
    }
  }
  SBAR();
#pragma unroll
  for (int k = 0; k < 2; ++k) {
#pragma unroll
    for (int d = 0; d < 4; ++d) {
      bf16x8 vf = __builtin_shufflevector(vlo[d][k], vhi[d][k], 0, 1, 2, 3, 4, 5, 6, 7);
      st.o[d] = mfma32(vf, pf[k], st.o[d]);
    }
  }
  if (NEXT) {
    const float pm = rowmax16(sn);
    SBAR();
    if (__builtin_expect(__any(pm > THR), 0)) {
      const float dl = fmaxf(pm, 0.f);
      const float alpha = __builtin_amdgcn_exp2f(-dl);
      st.m += dl;
      st.l *= alpha;
#pragma unroll
      for (int i = 0; i < 16; ++i) { sn[i] -= dl; st.negm[i] = -st.m; }
#pragma unroll
      for (int d = 0; d < 4; ++d)
#pragma unroll
        for (int i = 0; i < 16; ++i) st.o[d][i] *= alpha;
    }
    sc = sn;
  }
  SBAR();
}

__global__ __launch_bounds__(512, 2) void attn_kernel(const u16* __restrict__ q, const u16* __restrict__ kc, const u16* __restrict__ vc,
                                                      u16* __restrict__ og) {
  extern __shared__ __attribute__((aligned(16))) char lds[];
  const int bid = blockIdx.x;
  const int hkv = 2 * (bid & 7) + ((bid >> 3) & 1), b = bid >> 4;
  const int tid = threadIdx.x, lane = tid & 63, r = lane & 31, h = lane >> 5;
  const int wid = __builtin_amdgcn_readfirstlane(tid >> 6);
  const int kg = wid >> 2, g = (wid >> 1) & 1, pb = wid & 1;
  const int head = hkv * 2 + g;

  AttnCtx c;
  c.kbase = (const char*)(kc + (size_t)(b * HKV + hkv) * SK * DH);
  c.vbase = (const char*)(vc + (size_t)(b * HKV + hkv) * SK * DH);
  c.ldsw = wid * 8 * 256;
#pragma unroll
  for (int i = 0; i < 2; ++i) {
    const int row = 8 * wid + 4 * i + (lane >> 4), pos = lane & 15;
    c.koff[i] = row * 256 + ((pos ^ (row & 15)) << 4);
    c.voff[i] = row * 256 + ((pos ^ (((row & 3) << 2) | ((row >> 2) & 3))) << 4);
  }
  {
    const int krow = 32 * kg + r;
#pragma unroll
    for (int i = 0; i < 8; ++i) c.kaddr[i] = krow * 256 + (((2 * i + h) ^ (r & 15)) << 4);
    const int blk = (lane >> 4) & 1, q4 = (lane & 15) >> 2, p4 = lane & 3;
#pragma unroll
    for (int d = 0; d < 4; ++d)
#pragma unroll
      for (int t = 0; t < 2; ++t) {
        const int row = 32 * kg + 8 * t + 4 * h + q4;
        const int ch = 4 * d + 2 * blk + (p4 >> 1);
        const int sw = ((row & 3) << 2) | ((row >> 2) & 3);
        c.vaddr[2 * d + t] = 65536 + row * 256 + ((ch ^ sw) << 4) + 8 * (p4 & 1);
        asm volatile("" : "+v"(c.vaddr[2 * d + t]));
      }
  }
  issue_k<0>(c, 0);
  issue_k<1>(c, 1); issue_v<0>(c, 0);
  issue_k<2>(c, 2); issue_v<1>(c, 1);
  issue_k<3>(c, 3); issue_v<2>(c, 2);

  bf16x8 qf[8];
  {
    const u16* qrow = q + (size_t)(b * SQ + 32 * pb + r) * QW + head * DH + 8 * h;
#pragma unroll
    for (int i = 0; i < 8; ++i) qf[i] = *(const bf16x8*)(qrow + 16 * i);
  }
  asm volatile("s_waitcnt vmcnt(0)" ::: "memory");
#pragma unroll
  for (int i = 0; i < 8; ++i) {
    u32x4 w = __builtin_bit_cast(u32x4, qf[i]);
#pragma unroll
    for (int j = 0; j < 4; ++j) w[j] = cvtpk(__uint_as_float(w[j] << 16) * SC, __uint_as_float(w[j] & 0xffff0000u) * SC);
    qf[i] = __builtin_bit_cast(bf16x8, w);
  }

  AttnState st;
#pragma unroll
  for (int d = 0; d < 4; ++d) st.o[d] = f32x16{};
  st.m = -1e30f; st.l = 0.f;

  __builtin_amdgcn_s_barrier();
  f32x16 sc = {};
#pragma unroll
  for (int i = 0; i < 8; ++i) sc = mfma32(*(const bf16x8*)(lds + c.kaddr[i]), qf[i], sc);
  st.m = rowmax16(sc);
#pragma unroll
  for (int i = 0; i < 16; ++i) { sc[i] -= st.m; st.negm[i] = -st.m; }
#pragma unroll 1
  for (int t = 0; t < 12; t += 4) {
    step<0, 8, true, true, true>(c, st, sc, qf, lds, t);
    step<1, 8, true, true, true>(c, st, sc, qf, lds, t + 1);
    step<2, 8, true, true, true>(c, st, sc, qf, lds, t + 2);
    step<3, 8, true, true, true>(c, st, sc, qf, lds, t + 3);
  }
  step<0, 8, false, true, true>(c, st, sc, qf, lds, 12);
  step<1, 6, false, false, true>(c, st, sc, qf, lds, 13);
  step<2, 2, false, false, true>(c, st, sc, qf, lds, 14);
  step<3, 0, false, false, false>(c, st, sc, qf, lds, 15);

  float lt = st.l + __shfl_xor(st.l, 32);
  __syncthreads();
  char* xw = lds + wid * 8704;
  char* xp = lds + (wid ^ 4) * 8704;
  {
#pragma unroll
    for (int dd = 0; dd < 2; ++dd) {
      const f32x16 v = kg ? st.o[dd] : st.o[2 + dd];
#pragma unroll
      for (int a = 0; a < 4; ++a) {
        f32x4 w = {v[4 * a], v[4 * a + 1], v[4 * a + 2], v[4 * a + 3]};
        *(f32x4*)(xw + ((dd * 4 + a) * 64 + lane) * 16) = w;
      }
    }
    f32x2 ml = {st.m, lt};
    *(f32x2*)(xw + 8192 + lane * 8) = ml;
  }
  __syncthreads();
  f32x2 pml = *(const f32x2*)(xp + 8192 + lane * 8);
  const float mm = fmaxf(st.m, pml[0]);
  const float fa = __builtin_amdgcn_exp2f(st.m - mm), fb = __builtin_amdgcn_exp2f(pml[0] - mm);
  const float inv = 1.0f / (lt * fa + pml[1] * fb);
  const float wa = fa * inv, wb = fb * inv;
  char* stg = lds + 8 * 8704 + wid * 4608;
#pragma unroll
  for (int dd = 0; dd < 2; ++dd) {
    const f32x16 own = kg ? st.o[2 + dd] : st.o[dd];
#pragma unroll
    for (int a = 0; a < 4; ++a) {
      f32x4 pv = *(const f32x4*)(xp + ((dd * 4 + a) * 64 + lane) * 16);
      float o0 = own[4 * a] * wa + pv[0] * wb, o1 = own[4 * a + 1] * wa + pv[1] * wb;
      float o2 = own[4 * a + 2] * wa + pv[2] * wb, o3 = own[4 * a + 3] * wa + pv[3] * wb;
      u32x2 w = {cvtpk(o0, o1), cvtpk(o2, o3)};
      *(u32x2*)(stg + r * 144 + (32 * dd + 8 * a + 4 * h) * 2) = w;
    }
  }
  asm volatile("s_waitcnt lgkmcnt(0)" ::: "memory");
  __builtin_amdgcn_wave_barrier();
  u16* obase = og + (size_t)(b * SQ + 32 * pb) * QW + head * DH + kg * 64;
#pragma unroll
  for (int i = 0; i < 4; ++i) {
    const int row = i * 8 + (lane >> 3), ch = lane & 7;
    u32x4 w = *(const u32x4*)(stg + row * 144 + ch * 16);
    *(u32x4*)(obase + (size_t)row * QW + ch * 8) = w;
  }
}

extern "C" void kernel_launch(void* const* d_in, const int* in_sizes, int n_in, void* d_out, int out_size, void* d_ws, size_t ws_size,
                              hipStream_t stream) {
  const u16* x = (const u16*)d_in[0];
  const u16* kc = (const u16*)d_in[1];
  const u16* vc = (const u16*)d_in[2];
  const u16* wq = (const u16*)d_in[3];
  const u16* wo = (const u16*)d_in[4];
  u16* out = (u16*)d_out;
  u16* qws = (u16*)d_ws;
  u16* ows = qws + (size_t)MROWS * QW;
  hipFuncSetAttribute((const void*)attn_kernel, hipFuncAttributeMaxDynamicSharedMemorySize, ATT_LDS);
  gemm_simple<<<dim3(QW / 64, MROWS / 64), 256, 0, stream>>>(x, wq, qws, QW, DM);
  attn_kernel<<<256, 512, ATT_LDS, stream>>>(qws, kc, vc, ows);
  gemm_simple<<<dim3(DM / 64, MROWS / 64), 256, 0, stream>>>(ows, wo, out, DM, QW);
}
```

```cpp
#include <hip/hip_runtime.h>
#include <hip/hip_bf16.h>
#include <stdint.h>

typedef __attribute__((ext_vector_type(8))) short bf16x8;
typedef __attribute__((ext_vector_type(4))) short s16x4;
typedef __attribute__((ext_vector_type(16))) float f32x16;
typedef __attribute__((ext_vector_type(4))) float f32x4;
typedef __attribute__((ext_vector_type(2))) float f32x2;
typedef __attribute__((ext_vector_type(4))) unsigned u32x4;
typedef __attribute__((ext_vector_type(2))) unsigned u32x2;
typedef __attribute__((ext_vector_type(2))) __bf16 bf16x2_t;
typedef unsigned short u16;
typedef __attribute__((address_space(3))) s16x4* lds_s16x4_p;

#define DI __device__ __forceinline__

constexpr int NB = 16, SQ = 64, DM = 576, HQ = 32, HKV = 16, SK = 1024, DH = 128;
constexpr int MROWS = NB * SQ;
constexpr int QW = HQ * DH;

DI unsigned cvtpk(float lo, float hi) {
  f32x2 v = {lo, hi};
  bf16x2_t b = __builtin_convertvector(v, bf16x2_t);
  return __builtin_bit_cast(unsigned, b);
}
DI s16x4 tr_read(const char* p) {
  return __builtin_amdgcn_ds_read_tr16_b64_v4i16((lds_s16x4_p)(p));
}
DI f32x16 mfma32(bf16x8 a, bf16x8 b, f32x16 c) { return __builtin_amdgcn_mfma_f32_32x32x16_bf16(a, b, c, 0, 0, 0); }

__global__ __launch_bounds__(256) void gemm_simple(const u16* __restrict__ A, const u16* __restrict__ Bm, u16* __restrict__ C,
                                                   int N, int K) {
  __shared__ __attribute__((aligned(16))) char lds[64 * 80 + 32 * 192];
  char* As = lds;
  char* Bs = lds + 64 * 80;
  const int tid = threadIdx.x, wid = tid >> 6, lane = tid & 63, r = lane & 31, h = lane >> 5;
  const int m0 = blockIdx.y * 64, n0 = blockIdx.x * 64;
  const int wm = wid >> 1, wn = wid & 1;
  f32x16 acc = {};
  const int arow = tid >> 2, ach = tid & 3;
  const int brow = tid >> 3, bch = tid & 7;
  const int blk = (lane >> 4) & 1, q4 = (lane & 15) >> 2, p4 = lane & 3;
  for (int k0 = 0; k0 < K; k0 += 32) {
    bf16x8 av = *(const bf16x8*)(A + (size_t)(m0 + arow) * K + k0 + ach * 8);
    bf16x8 bv = *(const bf16x8*)(Bm + (size_t)(k0 + brow) * N + n0 + bch * 8);
    __syncthreads();
    *(bf16x8*)(As + arow * 80 + ach * 16) = av;
    *(bf16x8*)(Bs + brow * 192 + bch * 16) = bv;
    __syncthreads();
#pragma unroll
    for (int s = 0; s < 2; ++s) {
      bf16x8 xa = *(const bf16x8*)(As + (wm * 32 + r) * 80 + s * 32 + h * 16);
      s16x4 lo = tr_read(Bs + (16 * s + 8 * h + q4) * 192 + (wn * 32 + 16 * blk + 4 * p4) * 2);
      s16x4 hi = tr_read(Bs + (16 * s + 8 * h + 4 + q4) * 192 + (wn * 32 + 16 * blk + 4 * p4) * 2);
      bf16x8 wb = __builtin_shufflevector(lo, hi, 0, 1, 2, 3, 4, 5, 6, 7);
      acc = mfma32(wb, xa, acc);
    }
  }
  u16* crow = C + (size_t)(m0 + wm * 32 + r) * N + n0 + wn * 32;
#pragma unroll
  for (int a = 0; a < 4; ++a) {
    u32x2 w = {cvtpk(acc[4 * a], acc[4 * a + 1]), cvtpk(acc[4 * a + 2], acc[4 * a + 3])};
    *(u32x2*)(crow + 8 * a + 4 * h) = w;
  }
}

constexpr int NS = 4;
constexpr int SLOT = 32768;
constexpr int ATT_LDS = 163840;
constexpr float SC = 0.08838834764831845f * 1.4426950408889634f;

DI void dma16(unsigned ldsaddr, unsigned voff, const void* sbase) {
  unsigned keep;
  asm volatile("s_mov_b32 %0, m0\n\ts_mov_b32 m0, %1\n\ts_nop 0\n\tglobal_load_lds_dwordx4 %2, %3 nt\n\ts_mov_b32 m0, %0"
               : "=&s"(keep) : "s"(ldsaddr), "v"(voff), "s"(sbase) : "memory");
}

DI void dma16p(unsigned ldsaddr, unsigned voff, const void* sbase) {
  unsigned keep;
  asm volatile("s_mov_b32 %0, m0\n\ts_mov_b32 m0, %1\n\ts_nop 0\n\tglobal_load_lds_dwordx4 %2, %3\n\ts_mov_b32 m0, %0"
               : "=&s"(keep) : "s"(ldsaddr), "v"(voff), "s"(sbase) : "memory");
}

struct AttnState {
  f32x16 o[4];
  f32x16 negm;
  float m, l;
};

template <int N> DI void wait_vm() { asm volatile("s_waitcnt vmcnt(%0)" ::"i"(N) : "memory"); }

struct AttnCtx {
  const char* kbase;
  const char* vbase;
  unsigned koff[2], voff[2];
  unsigned ldsw;
  unsigned kaddr[8];
  unsigned vaddr[8];
};


#define SBAR() __builtin_amdgcn_sched_barrier(0)
constexpr float THR = 8.0f;
template <int SL> DI void issue_k(const AttnCtx& c, int t) {
  const char* kb = c.kbase + (size_t)t * 16384;
  unsigned l0 = SL * 16384 + c.ldsw;
  dma16(l0, c.koff[0], kb);
  dma16(l0 + 1024, c.koff[1], kb);
}
template <int SL> DI void issue_v(const AttnCtx& c, int t) {
  const char* vb = c.vbase + (size_t)t * 16384;
  unsigned l0 = 65536 + SL * 16384 + c.ldsw;
  dma16(l0, c.voff[0], vb);
  dma16(l0 + 1024, c.voff[1], vb);
}
DI float rowmax16(const f32x16& s) {
  float pm = s[0];
#pragma unroll
  for (int i = 1; i < 16; ++i) pm = fmaxf(pm, s[i]);
  auto rr = __builtin_amdgcn_permlane32_swap(__float_as_uint(pm), __float_as_uint(pm), false, false);
  return fmaxf(__uint_as_float(rr[0]), __uint_as_float(rr[1]));
}
template <int SL, int WAITN, bool ISSUEK, bool ISSUEV, bool NEXT> DI void step(const AttnCtx& c, AttnState& st, f32x16& sc, const bf16x8* qf, const char* lds, int t) {
  wait_vm<WAITN>();
  asm volatile("s_waitcnt lgkmcnt(0)" ::: "memory");
  __builtin_amdgcn_s_barrier();
  SBAR();
  const char* Ks = lds + ((SL + 1) & 3) * 16384;
  const char* Vs = lds + SL * 16384;
  bf16x8 kf[8];
  if (NEXT) {
#pragma unroll
    for (int i = 0; i < 8; ++i) kf[i] = *(const bf16x8*)(Ks + c.kaddr[i]);
  }
  SBAR();
  if (ISSUEK) issue_k<SL>(c, t + 4);
  if (ISSUEV) issue_v<(SL + 3) & 3>(c, t + 3);
  SBAR();
  f32x16 sn;
  if (NEXT) {
    asm("v_mfma_f32_32x32x16_bf16 %0, %1, %2, %3" : "=&v"(sn) : "v"(kf[0]), "v"(qf[0]), "v"(st.negm));
#pragma unroll
    for (int i = 1; i < 8; ++i) sn = mfma32(kf[i], qf[i], sn);
  }
  s16x4 vlo[4][2], vhi[4][2];
#pragma unroll
  for (int k = 0; k < 2; ++k)
#pragma unroll
    for (int d = 0; d < 4; ++d) {
      vlo[d][k] = tr_read(Vs + c.vaddr[2 * d] + k * 4096);
      vhi[d][k] = tr_read(Vs + c.vaddr[2 * d + 1] + k * 4096);
    }
  float ps = 0.f;
#pragma unroll
  for (int i = 0; i < 16; ++i) { sc[i] = __builtin_amdgcn_exp2f(sc[i]); ps += sc[i]; }
  st.l += ps;
  bf16x8 pf[2];
#pragma unroll
  for (int k = 0; k < 2; ++k) {
    u32x4 w = {cvtpk(sc[8 * k], sc[8 * k + 1]), cvtpk(sc[8 * k + 2], sc[8 * k + 3]), cvtpk(sc[8 * k + 4], sc[8 * k + 5]), cvtpk(sc[8 * k + 6], sc[8 * k + 7])};
    pf[k] = __builtin_bit_cast(bf16x8, w);
  }
  if (NEXT) {
#pragma unroll
    for (int i = 0; i < 8; ++i) {
      __builtin_amdgcn_sched_group_barrier(0x8, 1, 0);
      __builtin_amdgcn_sched_group_barrier(0x100, 2, 0);
      __builtin_amdgcn_sched_group_barrier(0x2, 5, 0);
    }
  }
  SBAR();
#pragma unroll
  for (int k = 0; k < 2; ++k) {
#pragma unroll
    for (int d = 0; d < 4; ++d) {
      bf16x8 vf = __builtin_shufflevector(vlo[d][k], vhi[d][k], 0, 1, 2, 3, 4, 5, 6, 7);
      st.o[d] = mfma32(vf, pf[k], st.o[d]);
    }
  }
  if (NEXT) {
    const float pm = rowmax16(sn);
    SBAR();
    if (__builtin_expect(__any(pm > THR), 0)) {
      const float dl = fmaxf(pm, 0.f);
      const float alpha = __builtin_amdgcn_exp2f(-dl);
      st.m += dl;
      st.l *= alpha;
#pragma unroll
      for (int i = 0; i < 16; ++i) { sn[i] -= dl; st.negm[i] = -st.m; }
#pragma unroll
      for (int d = 0; d < 4; ++d)
#pragma unroll
        for (int i = 0; i < 16; ++i) st.o[d][i] *= alpha;
    }
    sc = sn;
  }
  SBAR();
}

__global__ __launch_bounds__(512, 2) void attn_kernel(const u16* __restrict__ x, const u16* __restrict__ wq, const u16* __restrict__ kc,
                                                      const u16* __restrict__ vc, u16* __restrict__ og) {
  extern __shared__ __attribute__((aligned(16))) char lds[];
  const int bid = blockIdx.x;
  const int hkv = 2 * (bid & 7) + ((bid >> 3) & 1), b = bid >> 4;
  const int tid = threadIdx.x, lane = tid & 63, r = lane & 31, h = lane >> 5;
  const int wid = __builtin_amdgcn_readfirstlane(tid >> 6);
  const int kg = wid >> 2, g = (wid >> 1) & 1, pb = wid & 1;
  const int head = hkv * 2 + g;

  AttnCtx c;
  c.kbase = (const char*)(kc + (size_t)(b * HKV + hkv) * SK * DH);
  c.vbase = (const char*)(vc + (size_t)(b * HKV + hkv) * SK * DH);
  c.ldsw = wid * 8 * 256;
#pragma unroll
  for (int i = 0; i < 2; ++i) {
    const int row = 8 * wid + 4 * i + (lane >> 4), pos = lane & 15;
    c.koff[i] = row * 256 + ((pos ^ (row & 15)) << 4);
    c.voff[i] = row * 256 + ((pos ^ (((row & 3) << 2) | ((row >> 2) & 3))) << 4);
  }
  {
    const int krow = 32 * kg + r;
#pragma unroll
    for (int i = 0; i < 8; ++i) c.kaddr[i] = krow * 256 + (((2 * i + h) ^ (r & 15)) << 4);
    const int blk = (lane >> 4) & 1, q4 = (lane & 15) >> 2, p4 = lane & 3;
#pragma unroll
    for (int d = 0; d < 4; ++d)
#pragma unroll
      for (int t = 0; t < 2; ++t) {
        const int row = 32 * kg + 8 * t + 4 * h + q4;
        const int ch = 4 * d + 2 * blk + (p4 >> 1);
        const int sw = ((row & 3) << 2) | ((row >> 2) & 3);
        c.vaddr[2 * d + t] = 65536 + row * 256 + ((ch ^ sw) << 4) + 8 * (p4 & 1);
        asm volatile("" : "+v"(c.vaddr[2 * d + t]));
      }
  }
  const char* wqb = (const char*)(wq + (size_t)hkv * 256);
  const char* xb = (const char*)(x + (size_t)b * SQ * DM);
  constexpr unsigned WB0 = 49152, WB1 = 114688, WB2 = 131072, XB0 = 147456;
  unsigned wsrc[4], xsrc;
  unsigned warm;
  {
    const int k = (tid < 432 ? tid - 144 : 0) + 288 * (hkv & 1);
    const char* p = tid < 144 ? wqb + (size_t)(36 * b + (tid >> 2)) * 8192 + (tid & 3) * 128 : xb + (size_t)k * 128;
    asm volatile("global_load_dword %0, %1, off" : "=v"(warm) : "v"(p) : "memory");
  }
  if (wid >= 4) {
    const int w4 = wid - 4;
    unsigned ko[4], vo[4];
#pragma unroll
    for (int i = 0; i < 4; ++i) {
      const int row = 16 * w4 + 4 * i + (lane >> 4), pos = lane & 15;
      ko[i] = row * 256 + ((pos ^ (row & 15)) << 4);
      vo[i] = row * 256 + ((pos ^ (((row & 3) << 2) | ((row >> 2) & 3))) << 4);
    }
    const unsigned lw = w4 * 4096;
#pragma unroll
    for (int i = 0; i < 4; ++i) dma16(0 + lw + i * 1024, ko[i], c.kbase);
#pragma unroll
    for (int i = 0; i < 4; ++i) dma16(16384 + lw + i * 1024, ko[i], c.kbase + 16384);
#pragma unroll
    for (int i = 0; i < 4; ++i) dma16(65536 + lw + i * 1024, vo[i], c.vbase);
#pragma unroll
    for (int i = 0; i < 4; ++i) dma16(32768 + lw + i * 1024, ko[i], c.kbase + 32768);
#pragma unroll
    for (int i = 0; i < 4; ++i) dma16(65536 + 16384 + lw + i * 1024, vo[i], c.vbase + 16384);
#pragma unroll
    for (int i = 0; i < 4; ++i) dma16(65536 + 32768 + lw + i * 1024, vo[i], c.vbase + 32768);
  }
#pragma unroll
  for (int i = 0; i < 4; ++i) {
    const int row = 8 * (wid & 3) + 2 * i + (lane >> 5), pos = lane & 31;
    const int f = ((row & 3) << 2) | ((row >> 2) & 3);
    wsrc[i] = row * 8192 + ((pos ^ f) << 4);
  }
  {
    const int row = 16 * (wid & 3) + (lane >> 2), pos = lane & 3;
    xsrc = row * 1152 + ((pos ^ ((row >> 2) & 3)) << 4);
  }
  auto issue_chunk = [&](int ch, unsigned wbuf, unsigned xbuf) {
    const char* wsb = wqb + (size_t)ch * 32 * 8192;
#pragma unroll
    for (int i = 0; i < 4; ++i) dma16p(wbuf + (wid & 3) * 4096 + i * 1024, wsrc[i], wsb);
    dma16p(xbuf + (wid & 3) * 1024, xsrc, xb + ch * 64);
  };
  if (wid < 4) { issue_chunk(0, WB0, XB0); issue_chunk(1, WB1, XB0 + 4096); }
  unsigned waddr[2][2], xaddr[2];
  {
    const int blk = (lane >> 4) & 1, q4 = (lane & 15) >> 2, p4 = lane & 3;
#pragma unroll
    for (int j = 0; j < 2; ++j)
#pragma unroll
      for (int t = 0; t < 2; ++t) {
        const int row = 8 * h + 4 * t + q4;
        const int chn = 4 * (4 * g + 2 * kg + j) + 2 * blk + (p4 >> 1);
        const int f = ((row & 3) << 2) | ((row >> 2) & 3);
        waddr[j][t] = row * 512 + ((chn ^ f) << 4) + 8 * (p4 & 1);
      }
    const int row = 32 * pb + r;
#pragma unroll
    for (int s = 0; s < 2; ++s) xaddr[s] = row * 64 + (((2 * s + h) ^ ((row >> 2) & 3)) << 4);
  }
  f32x16 qa[2] = {};
  auto proj_chunk = [&](unsigned wbuf, unsigned xbuf) {
#pragma unroll
    for (int s = 0; s < 2; ++s) {
      const bf16x8 xf = *(const bf16x8*)(lds + xbuf + xaddr[s]);
#pragma unroll
      for (int j = 0; j < 2; ++j) {
        s16x4 lo = tr_read(lds + wbuf + s * 8192 + waddr[j][0]);
        s16x4 hi = tr_read(lds + wbuf + s * 8192 + waddr[j][1]);
        qa[j] = mfma32(__builtin_shufflevector(lo, hi, 0, 1, 2, 3, 4, 5, 6, 7), xf, qa[j]);
      }
    }
  };
#pragma unroll 1
  for (int ch = 0; ch < 18; ch += 3) {
    if (wid < 4) wait_vm<5>();
    asm volatile("s_waitcnt lgkmcnt(0)" ::: "memory");
    __builtin_amdgcn_s_barrier();
    if (wid < 4) issue_chunk(ch + 2, WB2, XB0 + 8192);
    proj_chunk(WB0, XB0);
    if (wid < 4) wait_vm<5>();
    asm volatile("s_waitcnt lgkmcnt(0)" ::: "memory");
    __builtin_amdgcn_s_barrier();
    if (wid < 4 && ch + 3 < 18) issue_chunk(ch + 3, WB0, XB0);
    proj_chunk(WB1, XB0 + 4096);
    if (wid < 4) { if (ch + 3 < 18) wait_vm<5>(); else wait_vm<0>(); }
    asm volatile("s_waitcnt lgkmcnt(0)" ::: "memory");
    __builtin_amdgcn_s_barrier();
    if (wid < 4 && ch + 4 < 18) issue_chunk(ch + 4, WB1, XB0 + 4096);
    proj_chunk(WB2, XB0 + 8192);
  }
  asm volatile("s_waitcnt lgkmcnt(0)" ::: "memory");
  __builtin_amdgcn_s_barrier();
  {
    char* qt = lds + 131072 + (g * 2 + pb) * 8192 + r * 256;
#pragma unroll
    for (int j = 0; j < 2; ++j)
#pragma unroll
      for (int a = 0; a < 4; ++a) {
        unsigned w0 = cvtpk(qa[j][4 * a], qa[j][4 * a + 1]), w1 = cvtpk(qa[j][4 * a + 2], qa[j][4 * a + 3]);
        w0 = cvtpk(__uint_as_float(w0 << 16) * SC, __uint_as_float(w0 & 0xffff0000u) * SC);
        w1 = cvtpk(__uint_as_float(w1 << 16) * SC, __uint_as_float(w1 & 0xffff0000u) * SC);
        u32x2 w = {w0, w1};
        *(u32x2*)(qt + (((8 * kg + 4 * j + a) ^ (r & 15)) << 4) + 8 * h) = w;
      }
  }
  issue_k<3>(c, 3);
  wait_vm<22>();
  asm volatile("s_waitcnt lgkmcnt(0)" :: "v"(warm) : "memory");
  __builtin_amdgcn_s_barrier();
  bf16x8 qf[8];
  {
    const char* qt = lds + 131072 + (g * 2 + pb) * 8192 + r * 256;
#pragma unroll
    for (int i = 0; i < 8; ++i) qf[i] = *(const bf16x8*)(qt + (((2 * i + h) ^ (r & 15)) << 4));
  }

  AttnState st;
#pragma unroll
  for (int d = 0; d < 4; ++d) st.o[d] = f32x16{};
  st.m = -1e30f; st.l = 0.f;

  f32x16 sc = {};
#pragma unroll
  for (int i = 0; i < 8; ++i) sc = mfma32(*(const bf16x8*)(lds + c.kaddr[i]), qf[i], sc);
  st.m = rowmax16(sc);
#pragma unroll
  for (int i = 0; i < 16; ++i) { sc[i] -= st.m; st.negm[i] = -st.m; }
  step<0, 14, true, true, true>(c, st, sc, qf, lds, 0);
  step<1, 10, true, true, true>(c, st, sc, qf, lds, 1);
  step<2, 8, true, true, true>(c, st, sc, qf, lds, 2);
  step<3, 8, true, true, true>(c, st, sc, qf, lds, 3);
#pragma unroll 1
  for (int t = 4; t < 12; t += 4) {
    step<0, 8, true, true, true>(c, st, sc, qf, lds, t);
    step<1, 8, true, true, true>(c, st, sc, qf, lds, t + 1);
    step<2, 8, true, true, true>(c, st, sc, qf, lds, t + 2);
    step<3, 8, true, true, true>(c, st, sc, qf, lds, t + 3);
  }
  step<0, 8, false, true, true>(c, st, sc, qf, lds, 12);
  step<1, 6, false, false, true>(c, st, sc, qf, lds, 13);
  step<2, 2, false, false, true>(c, st, sc, qf, lds, 14);
  step<3, 0, false, false, false>(c, st, sc, qf, lds, 15);

  float lt = st.l + __shfl_xor(st.l, 32);
  __syncthreads();
  char* xw = lds + wid * 8704;
  char* xp = lds + (wid ^ 4) * 8704;
  {
#pragma unroll
    for (int dd = 0; dd < 2; ++dd) {
      const f32x16 v = kg ? st.o[dd] : st.o[2 + dd];
#pragma unroll
      for (int a = 0; a < 4; ++a) {
        f32x4 w = {v[4 * a], v[4 * a + 1], v[4 * a + 2], v[4 * a + 3]};
        *(f32x4*)(xw + ((dd * 4 + a) * 64 + lane) * 16) = w;
      }
    }
    f32x2 ml = {st.m, lt};
    *(f32x2*)(xw + 8192 + lane * 8) = ml;
  }
  __syncthreads();
  f32x2 pml = *(const f32x2*)(xp + 8192 + lane * 8);
  const float mm = fmaxf(st.m, pml[0]);
  const float fa = __builtin_amdgcn_exp2f(st.m - mm), fb = __builtin_amdgcn_exp2f(pml[0] - mm);
  const float inv = 1.0f / (lt * fa + pml[1] * fb);
  const float wa = fa * inv, wb = fb * inv;
  char* stg = lds + 8 * 8704 + wid * 4608;
#pragma unroll
  for (int dd = 0; dd < 2; ++dd) {
    const f32x16 own = kg ? st.o[2 + dd] : st.o[dd];
#pragma unroll
    for (int a = 0; a < 4; ++a) {
      f32x4 pv = *(const f32x4*)(xp + ((dd * 4 + a) * 64 + lane) * 16);
      float o0 = own[4 * a] * wa + pv[0] * wb, o1 = own[4 * a + 1] * wa + pv[1] * wb;
      float o2 = own[4 * a + 2] * wa + pv[2] * wb, o3 = own[4 * a + 3] * wa + pv[3] * wb;
      u32x2 w = {cvtpk(o0, o1), cvtpk(o2, o3)};
      *(u32x2*)(stg + r * 144 + (32 * dd + 8 * a + 4 * h) * 2) = w;
    }
  }
  asm volatile("s_waitcnt lgkmcnt(0)" ::: "memory");
  __builtin_amdgcn_wave_barrier();
  u16* obase = og + (size_t)(b * SQ + 32 * pb) * QW + head * DH + kg * 64;
#pragma unroll
  for (int i = 0; i < 4; ++i) {
    const int row = i * 8 + (lane >> 3), ch = lane & 7;
    u32x4 w = *(const u32x4*)(stg + row * 144 + ch * 16);
    *(u32x4*)(obase + (size_t)row * QW + ch * 8) = w;
  }
}

extern "C" void kernel_launch(void* const* d_in, const int* in_sizes, int n_in, void* d_out, int out_size, void* d_ws, size_t ws_size,
                              hipStream_t stream) {
  const u16* x = (const u16*)d_in[0];
  const u16* kc = (const u16*)d_in[1];
  const u16* vc = (const u16*)d_in[2];
  const u16* wq = (const u16*)d_in[3];
  const u16* wo = (const u16*)d_in[4];
  u16* out = (u16*)d_out;
  u16* qws = (u16*)d_ws;
  u16* ows = qws + (size_t)MROWS * QW;
  hipFuncSetAttribute((const void*)attn_kernel, hipFuncAttributeMaxDynamicSharedMemorySize, ATT_LDS);
  attn_kernel<<<256, 512, ATT_LDS, stream>>>(x, wq, kc, vc, ows);
  gemm_simple<<<dim3(DM / 64, MROWS / 64), 256, 0, stream>>>(ows, wo, out, DM, QW);
}
```

```cpp
#define MAPPING 0
#include <hip/hip_runtime.h>
#include <hip/hip_bf16.h>
#include <stdint.h>

typedef __attribute__((ext_vector_type(8))) short bf16x8;
typedef __attribute__((ext_vector_type(4))) short s16x4;
typedef __attribute__((ext_vector_type(16))) float f32x16;
typedef __attribute__((ext_vector_type(4))) float f32x4;
typedef __attribute__((ext_vector_type(2))) float f32x2;
typedef __attribute__((ext_vector_type(4))) unsigned u32x4;
typedef __attribute__((ext_vector_type(2))) unsigned u32x2;
typedef __attribute__((ext_vector_type(2))) __bf16 bf16x2_t;
typedef unsigned short u16;
typedef __attribute__((address_space(3))) s16x4* lds_s16x4_p;

#define DI __device__ __forceinline__

constexpr int NB = 16, SQ = 64, DM = 576, HQ = 32, HKV = 16, SK = 1024, DH = 128;
constexpr int MROWS = NB * SQ;
constexpr int QW = HQ * DH;

DI unsigned cvtpk(float lo, float hi) {
  f32x2 v = {lo, hi};
  bf16x2_t b = __builtin_convertvector(v, bf16x2_t);
  return __builtin_bit_cast(unsigned, b);
}
DI s16x4 tr_read(const char* p) {
  return __builtin_amdgcn_ds_read_tr16_b64_v4i16((lds_s16x4_p)(p));
}
DI f32x16 mfma32(bf16x8 a, bf16x8 b, f32x16 c) { return __builtin_amdgcn_mfma_f32_32x32x16_bf16(a, b, c, 0, 0, 0); }

__global__ __launch_bounds__(256) void gemm_simple(const u16* __restrict__ A, const u16* __restrict__ Bm, u16* __restrict__ C,
                                                   int N, int K) {
  __shared__ __attribute__((aligned(16))) char lds[64 * 80 + 32 * 192];
  char* As = lds;
  char* Bs = lds + 64 * 80;
  const int tid = threadIdx.x, wid = tid >> 6, lane = tid & 63, r = lane & 31, h = lane >> 5;
  const int m0 = blockIdx.y * 64, n0 = blockIdx.x * 64;
  const int wm = wid >> 1, wn = wid & 1;
  f32x16 acc = {};
  const int arow = tid >> 2, ach = tid & 3;
  const int brow = tid >> 3, bch = tid & 7;
  const int blk = (lane >> 4) & 1, q4 = (lane & 15) >> 2, p4 = lane & 3;
  for (int k0 = 0; k0 < K; k0 += 32) {
    bf16x8 av = *(const bf16x8*)(A + (size_t)(m0 + arow) * K + k0 + ach * 8);
    bf16x8 bv = *(const bf16x8*)(Bm + (size_t)(k0 + brow) * N + n0 + bch * 8);
    __syncthreads();
    *(bf16x8*)(As + arow * 80 + ach * 16) = av;
    *(bf16x8*)(Bs + brow * 192 + bch * 16) = bv;
    __syncthreads();
#pragma unroll
    for (int s = 0; s < 2; ++s) {
      bf16x8 xa = *(const bf16x8*)(As + (wm * 32 + r) * 80 + s * 32 + h * 16);
      s16x4 lo = tr_read(Bs + (16 * s + 8 * h + q4) * 192 + (wn * 32 + 16 * blk + 4 * p4) * 2);
      s16x4 hi = tr_read(Bs + (16 * s + 8 * h + 4 + q4) * 192 + (wn * 32 + 16 * blk + 4 * p4) * 2);
      bf16x8 wb = __builtin_shufflevector(lo, hi, 0, 1, 2, 3, 4, 5, 6, 7);
      acc = mfma32(wb, xa, acc);
    }
  }
  u16* crow = C + (size_t)(m0 + wm * 32 + r) * N + n0 + wn * 32;
#pragma unroll
  for (int a = 0; a < 4; ++a) {
    u32x2 w = {cvtpk(acc[4 * a], acc[4 * a + 1]), cvtpk(acc[4 * a + 2], acc[4 * a + 3])};
    *(u32x2*)(crow + 8 * a + 4 * h) = w;
  }
}

constexpr int NS = 4;
constexpr int SLOT = 32768;
constexpr int ATT_LDS = 163840;
constexpr float SC = 0.08838834764831845f * 1.4426950408889634f;

DI void dma16(unsigned ldsaddr, unsigned voff, const void* sbase) {
  unsigned keep;
  asm volatile("s_mov_b32 %0, m0\n\ts_mov_b32 m0, %1\n\ts_nop 0\n\tglobal_load_lds_dwordx4 %2, %3 nt\n\ts_mov_b32 m0, %0"
               : "=&s"(keep) : "s"(ldsaddr), "v"(voff), "s"(sbase) : "memory");
}

DI void dma16p(unsigned ldsaddr, unsigned voff, const void* sbase) {
  unsigned keep;
  asm volatile("s_mov_b32 %0, m0\n\ts_mov_b32 m0, %1\n\ts_nop 0\n\tglobal_load_lds_dwordx4 %2, %3\n\ts_mov_b32 m0, %0"
               : "=&s"(keep) : "s"(ldsaddr), "v"(voff), "s"(sbase) : "memory");
}

struct AttnState {
  f32x16 o[4];
  f32x16 negm;
  float m, l;
};

template <int N> DI void wait_vm() { asm volatile("s_waitcnt vmcnt(%0)" ::"i"(N) : "memory"); }

struct AttnCtx {
  const char* kbase;
  const char* vbase;
  unsigned koff[2], voff[2];
  unsigned ldsw;
  unsigned kaddr[8];
  unsigned vaddr[8];
};

#define SBAR() __builtin_amdgcn_sched_barrier(0)
constexpr float THR = 8.0f;
template <int SL> DI void issue_k(const AttnCtx& c, int t) {
  const char* kb = c.kbase + (size_t)t * 16384;
  unsigned l0 = SL * 16384 + c.ldsw;
  dma16(l0, c.koff[0], kb);
  dma16(l0 + 1024, c.koff[1], kb);
}
template <int SL> DI void issue_v(const AttnCtx& c, int t) {
  const char* vb = c.vbase + (size_t)t * 16384;
  unsigned l0 = 65536 + SL * 16384 + c.ldsw;
  dma16(l0, c.voff[0], vb);
  dma16(l0 + 1024, c.voff[1], vb);
}
DI float rowmax16(const f32x16& s) {
  float pm = s[0];
#pragma unroll
  for (int i = 1; i < 16; ++i) pm = fmaxf(pm, s[i]);
  auto rr = __builtin_amdgcn_permlane32_swap(__float_as_uint(pm), __float_as_uint(pm), false, false);
  return fmaxf(__uint_as_float(rr[0]), __uint_as_float(rr[1]));
}
template <int SL, int WAITN, bool ISSUEK, bool ISSUEV, bool NEXT> DI void step(const AttnCtx& c, AttnState& st, f32x16& sc, const bf16x8* qf, const char* lds, int t) {
  wait_vm<WAITN>();
  asm volatile("s_waitcnt lgkmcnt(0)" ::: "memory");
  __builtin_amdgcn_s_barrier();
  SBAR();
  const char* Ks = lds + ((SL + 1) & 3) * 16384;
  const char* Vs = lds + SL * 16384;
  bf16x8 kf[8];
  if (NEXT) {
#pragma unroll
    for (int i = 0; i < 8; ++i) kf[i] = *(const bf16x8*)(Ks + c.kaddr[i]);
  }
  SBAR();
  if (ISSUEK) issue_k<SL>(c, t + 4);
  if (ISSUEV) issue_v<(SL + 3) & 3>(c, t + 3);
  SBAR();
  f32x16 sn;
  if (NEXT) {
    asm("v_mfma_f32_32x32x16_bf16 %0, %1, %2, %3" : "=&v"(sn) : "v"(kf[0]), "v"(qf[0]), "v"(st.negm));
#pragma unroll
    for (int i = 1; i < 8; ++i) sn = mfma32(kf[i], qf[i], sn);
  }
  s16x4 vlo[4][2], vhi[4][2];
#pragma unroll
  for (int k = 0; k < 2; ++k)
#pragma unroll
    for (int d = 0; d < 4; ++d) {
      vlo[d][k] = tr_read(Vs + c.vaddr[2 * d] + k * 4096);
      vhi[d][k] = tr_read(Vs + c.vaddr[2 * d + 1] + k * 4096);
    }
  float ps = 0.f;
#pragma unroll
  for (int i = 0; i < 16; ++i) { sc[i] = __builtin_amdgcn_exp2f(sc[i]); ps += sc[i]; }
  st.l += ps;
  bf16x8 pf[2];
#pragma unroll
  for (int k = 0; k < 2; ++k) {
    u32x4 w = {cvtpk(sc[8 * k], sc[8 * k + 1]), cvtpk(sc[8 * k + 2], sc[8 * k + 3]), cvtpk(sc[8 * k + 4], sc[8 * k + 5]), cvtpk(sc[8 * k + 6], sc[8 * k + 7])};
    pf[k] = __builtin_bit_cast(bf16x8, w);
  }
  if (NEXT) {
#pragma unroll
    for (int i = 0; i < 8; ++i) {
      __builtin_amdgcn_sched_group_barrier(0x8, 1, 0);
      __builtin_amdgcn_sched_group_barrier(0x100, 2, 0);
      __builtin_amdgcn_sched_group_barrier(0x2, 5, 0);
    }
  }
  SBAR();
#pragma unroll
  for (int k = 0; k < 2; ++k) {
#pragma unroll
    for (int d = 0; d < 4; ++d) {
      bf16x8 vf = __builtin_shufflevector(vlo[d][k], vhi[d][k], 0, 1, 2, 3, 4, 5, 6, 7);
      st.o[d] = mfma32(vf, pf[k], st.o[d]);
    }
  }
  if (NEXT) {
    const float pm = rowmax16(sn);
    SBAR();
    if (__builtin_expect(__any(pm > THR), 0)) {
      const float dl = fmaxf(pm, 0.f);
      const float alpha = __builtin_amdgcn_exp2f(-dl);
      st.m += dl;
      st.l *= alpha;
#pragma unroll
      for (int i = 0; i < 16; ++i) { sn[i] -= dl; st.negm[i] = -st.m; }
#pragma unroll
      for (int d = 0; d < 4; ++d)
#pragma unroll
        for (int i = 0; i < 16; ++i) st.o[d][i] *= alpha;
    }
    sc = sn;
  }
  SBAR();
}

__global__ __launch_bounds__(512, 2) void attn_kernel(const u16* __restrict__ x, const u16* __restrict__ wq, const u16* __restrict__ kc,
                                                      const u16* __restrict__ vc, u16* __restrict__ og) {
  extern __shared__ __attribute__((aligned(16))) char lds[];
  const int bid = blockIdx.x;
  const int hkv = 2 * (bid & 7) + ((bid >> 3) & 1), b = bid >> 4;
  const int rot = (b * 9) >> 3;
  const int tid = threadIdx.x, lane = tid & 63, r = lane & 31, h = lane >> 5;
  const int wid = __builtin_amdgcn_readfirstlane(tid >> 6);
  const int kg = wid >> 2, g = (wid >> 1) & 1, pb = wid & 1;
  const int head = hkv * 2 + g;

  AttnCtx c;
  c.kbase = (const char*)(kc + (size_t)(b * HKV + hkv) * SK * DH);
  c.vbase = (const char*)(vc + (size_t)(b * HKV + hkv) * SK * DH);
  c.ldsw = wid * 8 * 256;
#pragma unroll
  for (int i = 0; i < 2; ++i) {
    const int row = 8 * wid + 4 * i + (lane >> 4), pos = lane & 15;
    c.koff[i] = row * 256 + ((pos ^ (row & 15)) << 4);
    c.voff[i] = row * 256 + ((pos ^ (((row & 3) << 2) | ((row >> 2) & 3))) << 4);
  }
  {
    const int krow = 32 * kg + r;
#pragma unroll
    for (int i = 0; i < 8; ++i) c.kaddr[i] = krow * 256 + (((2 * i + h) ^ (r & 15)) << 4);
    const int blk = (lane >> 4) & 1, q4 = (lane & 15) >> 2, p4 = lane & 3;
#pragma unroll
    for (int d = 0; d < 4; ++d)
#pragma unroll
      for (int t = 0; t < 2; ++t) {
        const int row = 32 * kg + 8 * t + 4 * h + q4;
        const int ch = 4 * d + 2 * blk + (p4 >> 1);
        const int sw = ((row & 3) << 2) | ((row >> 2) & 3);
        c.vaddr[2 * d + t] = 65536 + row * 256 + ((ch ^ sw) << 4) + 8 * (p4 & 1);
        asm volatile("" : "+v"(c.vaddr[2 * d + t]));
      }
  }
  const char* wqb = (const char*)(wq + (size_t)hkv * 256);
  const char* xb = (const char*)(x + (size_t)b * SQ * DM);
  constexpr unsigned XB0 = 131072;
  unsigned wsrc[2], xsrc;
#pragma unroll
  for (int i = 0; i < 2; ++i) {
    const int row = 4 * wid + 2 * i + (lane >> 5), pos = lane & 31;
    const int f = ((row & 3) << 2) | ((row >> 2) & 3);
    wsrc[i] = row * 8192 + ((pos ^ f) << 4);
  }
  {
    const int row = 16 * (wid & 3) + (lane >> 2), pos = lane & 3;
    xsrc = row * 1152 + ((pos ^ ((row >> 2) & 3)) << 4);
  }
  u32x4 wv[18][2], xv[9];
#pragma unroll
  for (int j = 0; j < 9; ++j) {
#pragma unroll
    for (int e = 0; e < 2; ++e) {
      int ch = 2 * j + e + rot; if (ch >= 18) ch -= 18;
      const char* wsb = wqb + (size_t)ch * 32 * 8192;
#pragma unroll
      for (int i = 0; i < 2; ++i)
        asm volatile("global_load_dwordx4 %0, %1, %2 nt" : "=v"(wv[2 * j + e][i]) : "v"(wsrc[i]), "s"(wsb) : "memory");
    }
    int ch = 2 * j + kg + rot; if (ch >= 18) ch -= 18;
    asm volatile("global_load_dwordx4 %0, %1, %2 nt" : "=v"(xv[j]) : "v"(xsrc), "s"(xb + ch * 64) : "memory");
  }
  issue_k<0>(c, 0);
  issue_k<1>(c, 1); issue_v<0>(c, 0);
  issue_k<2>(c, 2); issue_v<1>(c, 1);
  issue_v<2>(c, 2);
  constexpr unsigned WB[2] = {49152, 114688};
  unsigned waddr[2][2], xaddr[2];
  {
    const int blk = (lane >> 4) & 1, q4 = (lane & 15) >> 2, p4 = lane & 3;
#pragma unroll
    for (int j = 0; j < 2; ++j)
#pragma unroll
      for (int t = 0; t < 2; ++t) {
        const int row = 8 * h + 4 * t + q4;
        const int chn = 4 * (4 * g + 2 * kg + j) + 2 * blk + (p4 >> 1);
        const int f = ((row & 3) << 2) | ((row >> 2) & 3);
        waddr[j][t] = row * 512 + ((chn ^ f) << 4) + 8 * (p4 & 1);
      }
    const int row = 32 * pb + r;
#pragma unroll
    for (int s = 0; s < 2; ++s) xaddr[s] = row * 64 + (((2 * s + h) ^ ((row >> 2) & 3)) << 4);
  }
  f32x16 qa[2] = {};
  auto proj_chunk = [&](unsigned wbuf, unsigned xbuf) {
#pragma unroll
    for (int s = 0; s < 2; ++s) {
      const bf16x8 xf = *(const bf16x8*)(lds + xbuf + xaddr[s]);
#pragma unroll
      for (int j = 0; j < 2; ++j) {
        s16x4 lo = tr_read(lds + wbuf + s * 8192 + waddr[j][0]);
        s16x4 hi = tr_read(lds + wbuf + s * 8192 + waddr[j][1]);
        qa[j] = mfma32(__builtin_shufflevector(lo, hi, 0, 1, 2, 3, 4, 5, 6, 7), xf, qa[j]);
      }
    }
  };
  auto stage_chunk = [&](int ch) {
    const int j = ch >> 1;
    if ((ch & 1) == 0)
      asm volatile("s_waitcnt vmcnt(%5)" : "+v"(wv[2 * j][0]), "+v"(wv[2 * j][1]), "+v"(wv[2 * j + 1][0]), "+v"(wv[2 * j + 1][1]), "+v"(xv[j]) : "i"(5 * (8 - j) + 12) : "memory");
    *(u32x4*)(lds + WB[ch & 1] + wid * 2048 + lane * 16) = wv[ch][0];
    *(u32x4*)(lds + WB[ch & 1] + wid * 2048 + 1024 + lane * 16) = wv[ch][1];
    if (kg == (ch & 1)) *(u32x4*)(lds + XB0 + (ch & 1) * 4096 + (wid & 3) * 1024 + lane * 16) = xv[j];
  };
  stage_chunk(0);
#pragma unroll
  for (int ch = 0; ch < 18; ++ch) {
    asm volatile("s_waitcnt lgkmcnt(0)" ::: "memory");
    __builtin_amdgcn_s_barrier();
    if (ch + 1 < 18) stage_chunk(ch + 1);
    proj_chunk(WB[ch & 1], XB0 + (ch & 1) * 4096);
  }
  asm volatile("s_waitcnt lgkmcnt(0)" ::: "memory");
  __builtin_amdgcn_s_barrier();
  {
    char* qt = lds + 131072 + (g * 2 + pb) * 8192 + r * 256;
#pragma unroll
    for (int j = 0; j < 2; ++j)
#pragma unroll
      for (int a = 0; a < 4; ++a) {
        unsigned w0 = cvtpk(qa[j][4 * a], qa[j][4 * a + 1]), w1 = cvtpk(qa[j][4 * a + 2], qa[j][4 * a + 3]);
        w0 = cvtpk(__uint_as_float(w0 << 16) * SC, __uint_as_float(w0 & 0xffff0000u) * SC);
        w1 = cvtpk(__uint_as_float(w1 << 16) * SC, __uint_as_float(w1 & 0xffff0000u) * SC);
        u32x2 w = {w0, w1};
        *(u32x2*)(qt + (((8 * kg + 4 * j + a) ^ (r & 15)) << 4) + 8 * h) = w;
      }
  }
  issue_k<3>(c, 3);
  wait_vm<12>();
  asm volatile("s_waitcnt lgkmcnt(0)" ::: "memory");
  __builtin_amdgcn_s_barrier();
  bf16x8 qf[8];
  {
    const char* qt = lds + 131072 + (g * 2 + pb) * 8192 + r * 256;
#pragma unroll
    for (int i = 0; i < 8; ++i) qf[i] = *(const bf16x8*)(qt + (((2 * i + h) ^ (r & 15)) << 4));
  }

  AttnState st;
#pragma unroll
  for (int d = 0; d < 4; ++d) st.o[d] = f32x16{};
  st.m = -1e30f; st.l = 0.f;

  f32x16 sc = {};
#pragma unroll
  for (int i = 0; i < 8; ++i) sc = mfma32(*(const bf16x8*)(lds + c.kaddr[i]), qf[i], sc);
  st.m = rowmax16(sc);
#pragma unroll
  for (int i = 0; i < 16; ++i) { sc[i] -= st.m; st.negm[i] = -st.m; }
  step<0, 8, true, true, true>(c, st, sc, qf, lds, 0);
  step<1, 8, true, true, true>(c, st, sc, qf, lds, 1);
  step<2, 8, true, true, true>(c, st, sc, qf, lds, 2);
  step<3, 8, true, true, true>(c, st, sc, qf, lds, 3);
#pragma unroll 1
  for (int t = 4; t < 12; t += 4) {
    step<0, 8, true, true, true>(c, st, sc, qf, lds, t);
    step<1, 8, true, true, true>(c, st, sc, qf, lds, t + 1);
    step<2, 8, true, true, true>(c, st, sc, qf, lds, t + 2);
    step<3, 8, true, true, true>(c, st, sc, qf, lds, t + 3);
  }
  step<0, 8, false, true, true>(c, st, sc, qf, lds, 12);
  step<1, 6, false, false, true>(c, st, sc, qf, lds, 13);
  step<2, 2, false, false, true>(c, st, sc, qf, lds, 14);
  step<3, 0, false, false, false>(c, st, sc, qf, lds, 15);

  float lt = st.l + __shfl_xor(st.l, 32);
  __syncthreads();
  char* xw = lds + wid * 8704;
  char* xp = lds + (wid ^ 4) * 8704;
  {
#pragma unroll
    for (int dd = 0; dd < 2; ++dd) {
      const f32x16 v = kg ? st.o[dd] : st.o[2 + dd];
#pragma unroll
      for (int a = 0; a < 4; ++a) {
        f32x4 w = {v[4 * a], v[4 * a + 1], v[4 * a + 2], v[4 * a + 3]};
        *(f32x4*)(xw + ((dd * 4 + a) * 64 + lane) * 16) = w;
      }
    }
    f32x2 ml = {st.m, lt};
    *(f32x2*)(xw + 8192 + lane * 8) = ml;
  }
  __syncthreads();
  f32x2 pml = *(const f32x2*)(xp + 8192 + lane * 8);
  const float mm = fmaxf(st.m, pml[0]);
  const float fa = __builtin_amdgcn_exp2f(st.m - mm), fb = __builtin_amdgcn_exp2f(pml[0] - mm);
  const float inv = 1.0f / (lt * fa + pml[1] * fb);
  const float wa = fa * inv, wb = fb * inv;
  char* stg = lds + 8 * 8704 + wid * 4608;
#pragma unroll
  for (int dd = 0; dd < 2; ++dd) {
    const f32x16 own = kg ? st.o[2 + dd] : st.o[dd];
#pragma unroll
    for (int a = 0; a < 4; ++a) {
      f32x4 pv = *(const f32x4*)(xp + ((dd * 4 + a) * 64 + lane) * 16);
      float o0 = own[4 * a] * wa + pv[0] * wb, o1 = own[4 * a + 1] * wa + pv[1] * wb;
      float o2 = own[4 * a + 2] * wa + pv[2] * wb, o3 = own[4 * a + 3] * wa + pv[3] * wb;
      u32x2 w = {cvtpk(o0, o1), cvtpk(o2, o3)};
      *(u32x2*)(stg + r * 144 + (32 * dd + 8 * a + 4 * h) * 2) = w;
    }
  }
  asm volatile("s_waitcnt lgkmcnt(0)" ::: "memory");
  __builtin_amdgcn_wave_barrier();
  u16* obase = og + (size_t)(b * SQ + 32 * pb) * QW + head * DH + kg * 64;
#pragma unroll
  for (int i = 0; i < 4; ++i) {
    const int row = i * 8 + (lane >> 3), ch = lane & 7;
    u32x4 w = *(const u32x4*)(stg + row * 144 + ch * 16);
    *(u32x4*)(obase + (size_t)row * QW + ch * 8) = w;
  }
}

extern "C" void kernel_launch(void* const* d_in, const int* in_sizes, int n_in, void* d_out, int out_size, void* d_ws, size_t ws_size,
                              hipStream_t stream) {
  const u16* x = (const u16*)d_in[0];
  const u16* kc = (const u16*)d_in[1];
  const u16* vc = (const u16*)d_in[2];
  const u16* wq = (const u16*)d_in[3];
  const u16* wo = (const u16*)d_in[4];
  u16* out = (u16*)d_out;
  u16* qws = (u16*)d_ws;
  u16* ows = qws + (size_t)MROWS * QW;
  hipFuncSetAttribute((const void*)attn_kernel, hipFuncAttributeMaxDynamicSharedMemorySize, ATT_LDS);
  attn_kernel<<<256, 512, ATT_LDS, stream>>>(x, wq, kc, vc, ows);
  gemm_simple<<<dim3(DM / 64, MROWS / 64), 256, 0, stream>>>(ows, wo, out, DM, QW);
}
```

```cpp
#define MAPPING 0
#include <hip/hip_runtime.h>
#include <hip/hip_bf16.h>
#include <stdint.h>

typedef __attribute__((ext_vector_type(8))) short bf16x8;
typedef __attribute__((ext_vector_type(4))) short s16x4;
typedef __attribute__((ext_vector_type(16))) float f32x16;
typedef __attribute__((ext_vector_type(4))) float f32x4;
typedef __attribute__((ext_vector_type(2))) float f32x2;
typedef __attribute__((ext_vector_type(4))) unsigned u32x4;
typedef __attribute__((ext_vector_type(2))) unsigned u32x2;
typedef __attribute__((ext_vector_type(2))) __bf16 bf16x2_t;
typedef unsigned short u16;
typedef __attribute__((address_space(3))) s16x4* lds_s16x4_p;

#define DI __device__ __forceinline__

constexpr int NB = 16, SQ = 64, DM = 576, HQ = 32, HKV = 16, SK = 1024, DH = 128;
constexpr int MROWS = NB * SQ;
constexpr int QW = HQ * DH;

DI unsigned cvtpk(float lo, float hi) {
  f32x2 v = {lo, hi};
  bf16x2_t b = __builtin_convertvector(v, bf16x2_t);
  return __builtin_bit_cast(unsigned, b);
}
DI s16x4 tr_read(const char* p) {
  return __builtin_amdgcn_ds_read_tr16_b64_v4i16((lds_s16x4_p)(p));
}
DI f32x16 mfma32(bf16x8 a, bf16x8 b, f32x16 c) { return __builtin_amdgcn_mfma_f32_32x32x16_bf16(a, b, c, 0, 0, 0); }

__global__ __launch_bounds__(256) void gemm_simple(const u16* __restrict__ A, const u16* __restrict__ Bm, u16* __restrict__ C,
                                                   int N, int K) {
  __shared__ __attribute__((aligned(16))) char lds[64 * 80 + 32 * 192];
  char* As = lds;
  char* Bs = lds + 64 * 80;
  const int tid = threadIdx.x, wid = tid >> 6, lane = tid & 63, r = lane & 31, h = lane >> 5;
  const int m0 = blockIdx.y * 64, n0 = blockIdx.x * 64;
  const int wm = wid >> 1, wn = wid & 1;
  f32x16 acc = {};
  const int arow = tid >> 2, ach = tid & 3;
  const int brow = tid >> 3, bch = tid & 7;
  const int blk = (lane >> 4) & 1, q4 = (lane & 15) >> 2, p4 = lane & 3;
  for (int k0 = 0; k0 < K; k0 += 32) {
    bf16x8 av = *(const bf16x8*)(A + (size_t)(m0 + arow) * K + k0 + ach * 8);
    bf16x8 bv = *(const bf16x8*)(Bm + (size_t)(k0 + brow) * N + n0 + bch * 8);
    __syncthreads();
    *(bf16x8*)(As + arow * 80 + ach * 16) = av;
    *(bf16x8*)(Bs + brow * 192 + bch * 16) = bv;
    __syncthreads();
#pragma unroll
    for (int s = 0; s < 2; ++s) {
      bf16x8 xa = *(const bf16x8*)(As + (wm * 32 + r) * 80 + s * 32 + h * 16);
      s16x4 lo = tr_read(Bs + (16 * s + 8 * h + q4) * 192 + (wn * 32 + 16 * blk + 4 * p4) * 2);
      s16x4 hi = tr_read(Bs + (16 * s + 8 * h + 4 + q4) * 192 + (wn * 32 + 16 * blk + 4 * p4) * 2);
      bf16x8 wb = __builtin_shufflevector(lo, hi, 0, 1, 2, 3, 4, 5, 6, 7);
      acc = mfma32(wb, xa, acc);
    }
  }
  u16* crow = C + (size_t)(m0 + wm * 32 + r) * N + n0 + wn * 32;
#pragma unroll
  for (int a = 0; a < 4; ++a) {
    u32x2 w = {cvtpk(acc[4 * a], acc[4 * a + 1]), cvtpk(acc[4 * a + 2], acc[4 * a + 3])};
    *(u32x2*)(crow + 8 * a + 4 * h) = w;
  }
}

constexpr int NS = 4;
constexpr int SLOT = 32768;
constexpr int ATT_LDS = 163840;
constexpr float SC = 0.08838834764831845f * 1.4426950408889634f;

DI void dma16(unsigned ldsaddr, unsigned voff, const void* sbase) {
  unsigned keep;
  asm volatile("s_mov_b32 %0, m0\n\ts_mov_b32 m0, %1\n\ts_nop 0\n\tglobal_load_lds_dwordx4 %2, %3 nt\n\ts_mov_b32 m0, %0"
               : "=&s"(keep) : "s"(ldsaddr), "v"(voff), "s"(sbase) : "memory");
}

DI void dma16p(unsigned ldsaddr, unsigned voff, const void* sbase) {
  unsigned keep;
  asm volatile("s_mov_b32 %0, m0\n\ts_mov_b32 m0, %1\n\ts_nop 0\n\tglobal_load_lds_dwordx4 %2, %3\n\ts_mov_b32 m0, %0"
               : "=&s"(keep) : "s"(ldsaddr), "v"(voff), "s"(sbase) : "memory");
}

struct AttnState {
  f32x16 o[4];
  f32x16 negm;
  float m, l;
};

template <int N> DI void wait_vm() { asm volatile("s_waitcnt vmcnt(%0)" ::"i"(N) : "memory"); }

struct AttnCtx {
  const char* kbase;
  const char* vbase;
  unsigned koff[2], voff[2];
  unsigned ldsw;
  unsigned kaddr[8];
  unsigned vaddr[8];
};

#define SBAR() __builtin_amdgcn_sched_barrier(0)
constexpr float THR = 8.0f;
template <int SL> DI void issue_k(const AttnCtx& c, int t) {
  const char* kb = c.kbase + (size_t)t * 16384;
  unsigned l0 = SL * 16384 + c.ldsw;
  dma16(l0, c.koff[0], kb);
  dma16(l0 + 1024, c.koff[1], kb);
}
template <int SL> DI void issue_v(const AttnCtx& c, int t) {
  const char* vb = c.vbase + (size_t)t * 16384;
  unsigned l0 = 65536 + SL * 16384 + c.ldsw;
  dma16(l0, c.voff[0], vb);
  dma16(l0 + 1024, c.voff[1], vb);
}
DI float rowmax16(const f32x16& s) {
  float pm = s[0];
#pragma unroll
  for (int i = 1; i < 16; ++i) pm = fmaxf(pm, s[i]);
  auto rr = __builtin_amdgcn_permlane32_swap(__float_as_uint(pm), __float_as_uint(pm), false, false);
  return fmaxf(__uint_as_float(rr[0]), __uint_as_float(rr[1]));
}
template <int SL, int WAITN, bool ISSUEK, bool ISSUEV, bool NEXT> DI void step(const AttnCtx& c, AttnState& st, f32x16& sc, const bf16x8* qf, const char* lds, int t) {
  wait_vm<WAITN>();
  asm volatile("s_waitcnt lgkmcnt(0)" ::: "memory");
  __builtin_amdgcn_s_barrier();
  SBAR();
  const char* Ks = lds + ((SL + 1) & 3) * 16384;
  const char* Vs = lds + SL * 16384;
  bf16x8 kf[8];
  if (NEXT) {
#pragma unroll
    for (int i = 0; i < 8; ++i) kf[i] = *(const bf16x8*)(Ks + c.kaddr[i]);
  }
  SBAR();
  if (ISSUEK) issue_k<SL>(c, t + 4);
  if (ISSUEV) issue_v<(SL + 3) & 3>(c, t + 3);
  SBAR();
  f32x16 sn;
  if (NEXT) {
    asm("v_mfma_f32_32x32x16_bf16 %0, %1, %2, %3" : "=&v"(sn) : "v"(kf[0]), "v"(qf[0]), "v"(st.negm));
#pragma unroll
    for (int i = 1; i < 8; ++i) sn = mfma32(kf[i], qf[i], sn);
  }
  s16x4 vlo[4][2], vhi[4][2];
#pragma unroll
  for (int k = 0; k < 2; ++k)
#pragma unroll
    for (int d = 0; d < 4; ++d) {
      vlo[d][k] = tr_read(Vs + c.vaddr[2 * d] + k * 4096);
      vhi[d][k] = tr_read(Vs + c.vaddr[2 * d + 1] + k * 4096);
    }
  float ps = 0.f;
#pragma unroll
  for (int i = 0; i < 16; ++i) { sc[i] = __builtin_amdgcn_exp2f(sc[i]); ps += sc[i]; }
  st.l += ps;
  bf16x8 pf[2];
#pragma unroll
  for (int k = 0; k < 2; ++k) {
    u32x4 w = {cvtpk(sc[8 * k], sc[8 * k + 1]), cvtpk(sc[8 * k + 2], sc[8 * k + 3]), cvtpk(sc[8 * k + 4], sc[8 * k + 5]), cvtpk(sc[8 * k + 6], sc[8 * k + 7])};
    pf[k] = __builtin_bit_cast(bf16x8, w);
  }
  if (NEXT) {
#pragma unroll
    for (int i = 0; i < 8; ++i) {
      __builtin_amdgcn_sched_group_barrier(0x8, 1, 0);
      __builtin_amdgcn_sched_group_barrier(0x100, 2, 0);
      __builtin_amdgcn_sched_group_barrier(0x2, 5, 0);
    }
  }
  SBAR();
#pragma unroll
  for (int k = 0; k < 2; ++k) {
#pragma unroll
    for (int d = 0; d < 4; ++d) {
      bf16x8 vf = __builtin_shufflevector(vlo[d][k], vhi[d][k], 0, 1, 2, 3, 4, 5, 6, 7);
      st.o[d] = mfma32(vf, pf[k], st.o[d]);
    }
  }
  if (NEXT) {
    const float pm = rowmax16(sn);
    SBAR();
    if (__builtin_expect(__any(pm > THR), 0)) {
      const float dl = fmaxf(pm, 0.f);
      const float alpha = __builtin_amdgcn_exp2f(-dl);
      st.m += dl;
      st.l *= alpha;
#pragma unroll
      for (int i = 0; i < 16; ++i) { sn[i] -= dl; st.negm[i] = -st.m; }
#pragma unroll
      for (int d = 0; d < 4; ++d)
#pragma unroll
        for (int i = 0; i < 16; ++i) st.o[d][i] *= alpha;
    }
    sc = sn;
  }
  SBAR();
}

__global__ __launch_bounds__(512, 2) void attn_kernel(const u16* __restrict__ x, const u16* __restrict__ wq, const u16* __restrict__ kc,
                                                      const u16* __restrict__ vc, u16* __restrict__ og) {
  extern __shared__ __attribute__((aligned(16))) char lds[];
  const int bid = blockIdx.x;
  const int hkv = 2 * (bid & 7) + ((bid >> 3) & 1), b = bid >> 4;
  const int rot = (b * 9) >> 3;
  const int tid = threadIdx.x, lane = tid & 63, r = lane & 31, h = lane >> 5;
  const int wid = __builtin_amdgcn_readfirstlane(tid >> 6);
  const int kg = wid >> 2, g = (wid >> 1) & 1, pb = wid & 1;
  const int head = hkv * 2 + g;

  AttnCtx c;
  c.kbase = (const char*)(kc + (size_t)(b * HKV + hkv) * SK * DH);
  c.vbase = (const char*)(vc + (size_t)(b * HKV + hkv) * SK * DH);
  c.ldsw = wid * 8 * 256;
#pragma unroll
  for (int i = 0; i < 2; ++i) {
    const int row = 8 * wid + 4 * i + (lane >> 4), pos = lane & 15;
    c.koff[i] = row * 256 + ((pos ^ (row & 15)) << 4);
    c.voff[i] = row * 256 + ((pos ^ (((row & 3) << 2) | ((row >> 2) & 3))) << 4);
  }
  {
    const int krow = 32 * kg + r;
#pragma unroll
    for (int i = 0; i < 8; ++i) c.kaddr[i] = krow * 256 + (((2 * i + h) ^ (r & 15)) << 4);
    const int blk = (lane >> 4) & 1, q4 = (lane & 15) >> 2, p4 = lane & 3;
#pragma unroll
    for (int d = 0; d < 4; ++d)
#pragma unroll
      for (int t = 0; t < 2; ++t) {
        const int row = 32 * kg + 8 * t + 4 * h + q4;
        const int ch = 4 * d + 2 * blk + (p4 >> 1);
        const int sw = ((row & 3) << 2) | ((row >> 2) & 3);
        c.vaddr[2 * d + t] = 65536 + row * 256 + ((ch ^ sw) << 4) + 8 * (p4 & 1);
        asm volatile("" : "+v"(c.vaddr[2 * d + t]));
      }
  }
  const char* wqb = (const char*)(wq + (size_t)hkv * 256);
  const char* xb = (const char*)(x + (size_t)b * SQ * DM);
  constexpr unsigned XB0 = 131072;
  unsigned wsrc[2], xsrc;
#pragma unroll
  for (int i = 0; i < 2; ++i) {
    const int row = 4 * wid + 2 * i + (lane >> 5), pos = lane & 31;
    const int f = ((row & 3) << 2) | ((row >> 2) & 3);
    wsrc[i] = row * 8192 + ((pos ^ f) << 4);
  }
  {
    const int row = 16 * (wid & 3) + (lane >> 2), pos = lane & 3;
    xsrc = row * 1152 + ((pos ^ ((row >> 2) & 3)) << 4);
  }
  auto issue_chunk = [&](int chi, unsigned wbuf, unsigned xbuf) {
    int ch = chi + rot; if (ch >= 18) ch -= 18;
    const char* wsb = wqb + (size_t)ch * 32 * 8192;
#pragma unroll
    for (int i = 0; i < 2; ++i) dma16p(wbuf + wid * 2048 + i * 1024, wsrc[i], wsb);
    if (wid < 4) dma16p(xbuf + wid * 1024, xsrc, xb + ch * 64);
  };
  constexpr unsigned PS[8] = {6, 7, 0, 1, 4, 2, 5, 3};
#pragma unroll
  for (int ci = 0; ci < 8; ++ci) issue_chunk(ci, PS[ci] * 16384, XB0 + ci * 4096);
  unsigned waddr[2][2], xaddr[2];
  {
    const int blk = (lane >> 4) & 1, q4 = (lane & 15) >> 2, p4 = lane & 3;
#pragma unroll
    for (int j = 0; j < 2; ++j)
#pragma unroll
      for (int t = 0; t < 2; ++t) {
        const int row = 8 * h + 4 * t + q4;
        const int chn = 4 * (4 * g + 2 * kg + j) + 2 * blk + (p4 >> 1);
        const int f = ((row & 3) << 2) | ((row >> 2) & 3);
        waddr[j][t] = row * 512 + ((chn ^ f) << 4) + 8 * (p4 & 1);
      }
    const int row = 32 * pb + r;
#pragma unroll
    for (int s = 0; s < 2; ++s) xaddr[s] = row * 64 + (((2 * s + h) ^ ((row >> 2) & 3)) << 4);
  }
  f32x16 qa[2] = {};
  auto proj_chunk = [&](unsigned wbuf, unsigned xbuf) {
#pragma unroll
    for (int s = 0; s < 2; ++s) {
      const bf16x8 xf = *(const bf16x8*)(lds + xbuf + xaddr[s]);
#pragma unroll
      for (int j = 0; j < 2; ++j) {
        s16x4 lo = tr_read(lds + wbuf + s * 8192 + waddr[j][0]);
        s16x4 hi = tr_read(lds + wbuf + s * 8192 + waddr[j][1]);
        qa[j] = mfma32(__builtin_shufflevector(lo, hi, 0, 1, 2, 3, 4, 5, 6, 7), xf, qa[j]);
      }
    }
  };
#pragma unroll
  for (int ch = 0; ch < 18; ++ch) {
    {
      const int yc = ch == 0 ? 7 : ((ch + 6 <= 17 ? ch + 6 : 17) - ch);
      const int yt = ch > 11 ? ch - 11 : 0;
      if (wid < 4) asm volatile("s_waitcnt vmcnt(%0)" ::"i"(3 * yc + 2 * yt) : "memory");
      else asm volatile("s_waitcnt vmcnt(%0)" ::"i"(2 * yc + 2 * yt) : "memory");
    }
    asm volatile("s_waitcnt lgkmcnt(0)" ::: "memory");
    __builtin_amdgcn_s_barrier();
    if (ch >= 1 && ch <= 10) issue_chunk(ch + 7, PS[(ch + 7) & 7] * 16384, XB0 + ((ch + 7) & 7) * 4096);
    if (ch == 11) issue_k<0>(c, 0);
    if (ch == 12) issue_k<1>(c, 1);
    if (ch == 13) issue_v<0>(c, 0);
    if (ch == 14) issue_k<2>(c, 2);
    if (ch == 15) issue_v<1>(c, 1);
    if (ch == 16) issue_k<3>(c, 3);
    if (ch == 17) issue_v<2>(c, 2);
    proj_chunk(PS[ch & 7] * 16384, XB0 + (ch & 7) * 4096);
  }
  asm volatile("s_waitcnt lgkmcnt(0)" ::: "memory");
  __builtin_amdgcn_s_barrier();
  {
    char* qt = lds + 131072 + (g * 2 + pb) * 8192 + r * 256;
#pragma unroll
    for (int j = 0; j < 2; ++j)
#pragma unroll
      for (int a = 0; a < 4; ++a) {
        unsigned w0 = cvtpk(qa[j][4 * a], qa[j][4 * a + 1]), w1 = cvtpk(qa[j][4 * a + 2], qa[j][4 * a + 3]);
        w0 = cvtpk(__uint_as_float(w0 << 16) * SC, __uint_as_float(w0 & 0xffff0000u) * SC);
        w1 = cvtpk(__uint_as_float(w1 << 16) * SC, __uint_as_float(w1 & 0xffff0000u) * SC);
        u32x2 w = {w0, w1};
        *(u32x2*)(qt + (((8 * kg + 4 * j + a) ^ (r & 15)) << 4) + 8 * h) = w;
      }
  }
  wait_vm<12>();
  asm volatile("s_waitcnt lgkmcnt(0)" ::: "memory");
  __builtin_amdgcn_s_barrier();
  bf16x8 qf[8];
  {
    const char* qt = lds + 131072 + (g * 2 + pb) * 8192 + r * 256;
#pragma unroll
    for (int i = 0; i < 8; ++i) qf[i] = *(const bf16x8*)(qt + (((2 * i + h) ^ (r & 15)) << 4));
  }

  AttnState st;
#pragma unroll
  for (int d = 0; d < 4; ++d) st.o[d] = f32x16{};
  st.m = -1e30f; st.l = 0.f;

  f32x16 sc = {};
#pragma unroll
  for (int i = 0; i < 8; ++i) sc = mfma32(*(const bf16x8*)(lds + c.kaddr[i]), qf[i], sc);
  st.m = rowmax16(sc);
#pragma unroll
  for (int i = 0; i < 16; ++i) { sc[i] -= st.m; st.negm[i] = -st.m; }
  step<0, 8, true, true, true>(c, st, sc, qf, lds, 0);
  step<1, 8, true, true, true>(c, st, sc, qf, lds, 1);
  step<2, 8, true, true, true>(c, st, sc, qf, lds, 2);
  step<3, 8, true, true, true>(c, st, sc, qf, lds, 3);
#pragma unroll 1
  for (int t = 4; t < 12; t += 4) {
    step<0, 8, true, true, true>(c, st, sc, qf, lds, t);
    step<1, 8, true, true, true>(c, st, sc, qf, lds, t + 1);
    step<2, 8, true, true, true>(c, st, sc, qf, lds, t + 2);
    step<3, 8, true, true, true>(c, st, sc, qf, lds, t + 3);
  }
  step<0, 8, false, true, true>(c, st, sc, qf, lds, 12);
  step<1, 6, false, false, true>(c, st, sc, qf, lds, 13);
  step<2, 2, false, false, true>(c, st, sc, qf, lds, 14);
  step<3, 0, false, false, false>(c, st, sc, qf, lds, 15);

  float lt = st.l + __shfl_xor(st.l, 32);
  __syncthreads();
  char* xw = lds + wid * 8704;
  char* xp = lds + (wid ^ 4) * 8704;
  {
#pragma unroll
    for (int dd = 0; dd < 2; ++dd) {
      const f32x16 v = kg ? st.o[dd] : st.o[2 + dd];
#pragma unroll
      for (int a = 0; a < 4; ++a) {
        f32x4 w = {v[4 * a], v[4 * a + 1], v[4 * a + 2], v[4 * a + 3]};
        *(f32x4*)(xw + ((dd * 4 + a) * 64 + lane) * 16) = w;
      }
    }
    f32x2 ml = {st.m, lt};
    *(f32x2*)(xw + 8192 + lane * 8) = ml;
  }
  __syncthreads();
  f32x2 pml = *(const f32x2*)(xp + 8192 + lane * 8);
  const float mm = fmaxf(st.m, pml[0]);
  const float fa = __builtin_amdgcn_exp2f(st.m - mm), fb = __builtin_amdgcn_exp2f(pml[0] - mm);
  const float inv = 1.0f / (lt * fa + pml[1] * fb);
  const float wa = fa * inv, wb = fb * inv;
  char* stg = lds + 8 * 8704 + wid * 4608;
#pragma unroll
  for (int dd = 0; dd < 2; ++dd) {
    const f32x16 own = kg ? st.o[2 + dd] : st.o[dd];
#pragma unroll
    for (int a = 0; a < 4; ++a) {
      f32x4 pv = *(const f32x4*)(xp + ((dd * 4 + a) * 64 + lane) * 16);
      float o0 = own[4 * a] * wa + pv[0] * wb, o1 = own[4 * a + 1] * wa + pv[1] * wb;
      float o2 = own[4 * a + 2] * wa + pv[2] * wb, o3 = own[4 * a + 3] * wa + pv[3] * wb;
      u32x2 w = {cvtpk(o0, o1), cvtpk(o2, o3)};
      *(u32x2*)(stg + r * 144 + (32 * dd + 8 * a + 4 * h) * 2) = w;
    }
  }
  asm volatile("s_waitcnt lgkmcnt(0)" ::: "memory");
  __builtin_amdgcn_wave_barrier();
  u16* obase = og + (size_t)(b * SQ + 32 * pb) * QW + head * DH + kg * 64;
#pragma unroll
  for (int i = 0; i < 4; ++i) {
    const int row = i * 8 + (lane >> 3), ch = lane & 7;
    u32x4 w = *(const u32x4*)(stg + row * 144 + ch * 16);
    *(u32x4*)(obase + (size_t)row * QW + ch * 8) = w;
  }
}

DI f32x4 mfma16(bf16x8 a, bf16x8 b, f32x4 c) { return __builtin_amdgcn_mfma_f32_16x16x32_bf16(a, b, c, 0, 0, 0); }
constexpr int OP_GSTRIDE = 2432;
constexpr int OP_LDS = 64 * OP_GSTRIDE;
__global__ __launch_bounds__(512, 2) void oproj_kernel(const u16* __restrict__ og, const u16* __restrict__ wo, float* __restrict__ part) {
  extern __shared__ __attribute__((aligned(16))) char lds[];
  const int bid = blockIdx.x;
  const int ks = bid & 7, nt = (bid >> 3) & 3, mt = bid >> 5;
  const int tid = threadIdx.x, lane = tid & 63;
  const int wid = __builtin_amdgcn_readfirstlane(tid >> 6);
  const int mq = wid & 3, kh = wid >> 2;
  const int l15 = lane & 15, g4 = lane >> 4;
  const char* ob = (const char*)(og + (size_t)(128 * mt + 32 * mq) * QW + 512 * ks + 256 * kh);
  const unsigned ooff0 = l15 * 8192 + g4 * 16, ooff1 = ooff0 + 16 * 8192;
  const char* wb = (const char*)(wo + (size_t)(512 * ks) * DM + 144 * nt);
  auto wsrc_of = [&](int p) -> unsigned {
    const int idx = p * 64 + lane;
    const int grp = idx / 152, rem = idx - grp * 152;
    const int rr = rem / 18, ch = rem - rr * 18;
    return rem < 144 ? (unsigned)((8 * grp + rr) * 1152 + ch * 16) : 0u;
  };
  bf16x8 of[2][8];
  auto load_o = [&](int s) {
    asm volatile("global_load_dwordx4 %0, %1, %2 offset:%3" : "=v"(of[0][s]) : "v"(ooff0), "s"(ob), "i"(s * 64) : "memory");
    asm volatile("global_load_dwordx4 %0, %1, %2 offset:%3" : "=v"(of[1][s]) : "v"(ooff1), "s"(ob), "i"(s * 64) : "memory");
  };
  auto dma_quarter = [&](int qd) {
#pragma unroll
    for (int i = 0; i < 5; ++i) {
      const int pl = wid + 8 * i;
      if (pl < 38) { const int p = 38 * qd + pl; dma16p(p * 1024, wsrc_of(p), wb); }
    }
  };
#pragma unroll
  for (int s = 0; s < 4; ++s) load_o(s);
  dma_quarter(0); dma_quarter(2);
#pragma unroll
  for (int s = 4; s < 8; ++s) load_o(s);
  dma_quarter(1); dma_quarter(3);

  f32x4 acc[2][9];
#pragma unroll
  for (int i = 0; i < 2; ++i)
#pragma unroll
    for (int c = 0; c < 9; ++c) acc[i][c] = f32x4{0.f, 0.f, 0.f, 0.f};
  const int q4 = (lane & 15) >> 2, p4 = lane & 3;
  const unsigned abase = (unsigned)((32 * kh + g4) * OP_GSTRIDE + q4 * 288 + p4 * 8);
  auto compute_half = [&](int hf) {
#pragma unroll
    for (int s = 4 * hf; s < 4 * hf + 4; ++s) {
      const char* ap = lds + abase + s * 4 * OP_GSTRIDE;
#pragma unroll
      for (int c = 0; c < 9; ++c) {
        s16x4 lo = tr_read(ap + c * 32), hi = tr_read(ap + c * 32 + 4 * 288);
        bf16x8 af = __builtin_shufflevector(lo, hi, 0, 1, 2, 3, 4, 5, 6, 7);
        acc[0][c] = mfma16(af, of[0][s], acc[0][c]);
        acc[1][c] = mfma16(af, of[1][s], acc[1][c]);
      }
    }
  };
  asm volatile("s_waitcnt vmcnt(16)" : "+v"(of[0][0]), "+v"(of[0][1]), "+v"(of[0][2]), "+v"(of[0][3]), "+v"(of[1][0]), "+v"(of[1][1]), "+v"(of[1][2]), "+v"(of[1][3]) :: "memory");
  __builtin_amdgcn_s_barrier();
  compute_half(0);
  asm volatile("s_waitcnt vmcnt(0)" : "+v"(of[0][4]), "+v"(of[0][5]), "+v"(of[0][6]), "+v"(of[0][7]), "+v"(of[1][4]), "+v"(of[1][5]), "+v"(of[1][6]), "+v"(of[1][7]) :: "memory");
  __builtin_amdgcn_s_barrier();
  compute_half(1);
  __syncthreads();
  {
    char* xs = lds + wid * 9216 + lane * 16;
#pragma unroll
    for (int c = 0; c < 9; ++c) *(f32x4*)(xs + c * 1024) = kh ? acc[0][c] : acc[1][c];
  }
  __syncthreads();
  {
    const char* xr = lds + (wid ^ 4) * 9216 + lane * 16;
    float* pp = part + ((size_t)ks * MROWS + 128 * mt + 32 * mq + 16 * kh + l15) * DM + 144 * nt + 4 * g4;
#pragma unroll
    for (int c = 0; c < 9; ++c) {
      f32x4 v = *(const f32x4*)(xr + c * 1024);
      f32x4 o = kh ? acc[1][c] : acc[0][c];
      *(f32x4*)(pp + 16 * c) = v + o;
    }
  }
}

__global__ __launch_bounds__(256) void reduce_kernel(const float* __restrict__ part, u16* __restrict__ out) {
  const int i = blockIdx.x * 256 + threadIdx.x;
  f32x4 s = *(const f32x4*)(part + (size_t)i * 4);
#pragma unroll
  for (int k = 1; k < 8; ++k) s += *(const f32x4*)(part + (size_t)k * MROWS * DM + (size_t)i * 4);
  u32x2 w = {cvtpk(s[0], s[1]), cvtpk(s[2], s[3])};
  *(u32x2*)(out + (size_t)i * 4) = w;
}

extern "C" void kernel_launch(void* const* d_in, const int* in_sizes, int n_in, void* d_out, int out_size, void* d_ws, size_t ws_size,
                              hipStream_t stream) {
  const u16* x = (const u16*)d_in[0];
  const u16* kc = (const u16*)d_in[1];
  const u16* vc = (const u16*)d_in[2];
  const u16* wq = (const u16*)d_in[3];
  const u16* wo = (const u16*)d_in[4];
  u16* out = (u16*)d_out;
  u16* qws = (u16*)d_ws;
  u16* ows = qws + (size_t)MROWS * QW;
  hipFuncSetAttribute((const void*)attn_kernel, hipFuncAttributeMaxDynamicSharedMemorySize, ATT_LDS);
  attn_kernel<<<256, 512, ATT_LDS, stream>>>(x, wq, kc, vc, ows);
  float* part = (float*)((char*)d_ws + (32 << 20));
  hipFuncSetAttribute((const void*)oproj_kernel, hipFuncAttributeMaxDynamicSharedMemorySize, OP_LDS);
  oproj_kernel<<<256, 512, OP_LDS, stream>>>(ows, wo, part);
  reduce_kernel<<<MROWS * DM / 4 / 256, 256, 0, stream>>>(part, out);
}
```

```cpp
#define MAPPING 0
#include <hip/hip_runtime.h>
#include <hip/hip_bf16.h>
#include <stdint.h>

typedef __attribute__((ext_vector_type(8))) short bf16x8;
typedef __attribute__((ext_vector_type(4))) short s16x4;
typedef __attribute__((ext_vector_type(16))) float f32x16;
typedef __attribute__((ext_vector_type(4))) float f32x4;
typedef __attribute__((ext_vector_type(2))) float f32x2;
typedef __attribute__((ext_vector_type(4))) unsigned u32x4;
typedef __attribute__((ext_vector_type(2))) unsigned u32x2;
typedef __attribute__((ext_vector_type(2))) __bf16 bf16x2_t;
typedef unsigned short u16;
typedef __attribute__((address_space(3))) s16x4* lds_s16x4_p;

#define DI __device__ __forceinline__

constexpr int NB = 16, SQ = 64, DM = 576, HQ = 32, HKV = 16, SK = 1024, DH = 128;
constexpr int MROWS = NB * SQ;
constexpr int QW = HQ * DH;

DI unsigned cvtpk(float lo, float hi) {
  f32x2 v = {lo, hi};
  bf16x2_t b = __builtin_convertvector(v, bf16x2_t);
  return __builtin_bit_cast(unsigned, b);
}
DI s16x4 tr_read(const char* p) {
  return __builtin_amdgcn_ds_read_tr16_b64_v4i16((lds_s16x4_p)(p));
}
DI f32x16 mfma32(bf16x8 a, bf16x8 b, f32x16 c) { return __builtin_amdgcn_mfma_f32_32x32x16_bf16(a, b, c, 0, 0, 0); }

__global__ __launch_bounds__(256) void gemm_simple(const u16* __restrict__ A, const u16* __restrict__ Bm, u16* __restrict__ C,
                                                   int N, int K) {
  __shared__ __attribute__((aligned(16))) char lds[64 * 80 + 32 * 192];
  char* As = lds;
  char* Bs = lds + 64 * 80;
  const int tid = threadIdx.x, wid = tid >> 6, lane = tid & 63, r = lane & 31, h = lane >> 5;
  const int m0 = blockIdx.y * 64, n0 = blockIdx.x * 64;
  const int wm = wid >> 1, wn = wid & 1;
  f32x16 acc = {};
  const int arow = tid >> 2, ach = tid & 3;
  const int brow = tid >> 3, bch = tid & 7;
  const int blk = (lane >> 4) & 1, q4 = (lane & 15) >> 2, p4 = lane & 3;
  for (int k0 = 0; k0 < K; k0 += 32) {
    bf16x8 av = *(const bf16x8*)(A + (size_t)(m0 + arow) * K + k0 + ach * 8);
    bf16x8 bv = *(const bf16x8*)(Bm + (size_t)(k0 + brow) * N + n0 + bch * 8);
    __syncthreads();
    *(bf16x8*)(As + arow * 80 + ach * 16) = av;
    *(bf16x8*)(Bs + brow * 192 + bch * 16) = bv;
    __syncthreads();
#pragma unroll
    for (int s = 0; s < 2; ++s) {
      bf16x8 xa = *(const bf16x8*)(As + (wm * 32 + r) * 80 + s * 32 + h * 16);
      s16x4 lo = tr_read(Bs + (16 * s + 8 * h + q4) * 192 + (wn * 32 + 16 * blk + 4 * p4) * 2);
      s16x4 hi = tr_read(Bs + (16 * s + 8 * h + 4 + q4) * 192 + (wn * 32 + 16 * blk + 4 * p4) * 2);
      bf16x8 wb = __builtin_shufflevector(lo, hi, 0, 1, 2, 3, 4, 5, 6, 7);
      acc = mfma32(wb, xa, acc);
    }
  }
  u16* crow = C + (size_t)(m0 + wm * 32 + r) * N + n0 + wn * 32;
#pragma unroll
  for (int a = 0; a < 4; ++a) {
    u32x2 w = {cvtpk(acc[4 * a], acc[4 * a + 1]), cvtpk(acc[4 * a + 2], acc[4 * a + 3])};
    *(u32x2*)(crow + 8 * a + 4 * h) = w;
  }
}

constexpr int NS = 4;
constexpr int SLOT = 32768;
constexpr int ATT_LDS = 163840;
constexpr float SC = 0.08838834764831845f * 1.4426950408889634f;

DI void dma16(unsigned ldsaddr, unsigned voff, const void* sbase) {
  unsigned keep;
  asm volatile("s_mov_b32 %0, m0\n\ts_mov_b32 m0, %1\n\ts_nop 0\n\tglobal_load_lds_dwordx4 %2, %3 nt\n\ts_mov_b32 m0, %0"
               : "=&s"(keep) : "s"(ldsaddr), "v"(voff), "s"(sbase) : "memory");
}

DI void dma16p(unsigned ldsaddr, unsigned voff, const void* sbase) {
  unsigned keep;
  asm volatile("s_mov_b32 %0, m0\n\ts_mov_b32 m0, %1\n\ts_nop 0\n\tglobal_load_lds_dwordx4 %2, %3\n\ts_mov_b32 m0, %0"
               : "=&s"(keep) : "s"(ldsaddr), "v"(voff), "s"(sbase) : "memory");
}

struct AttnState {
  f32x16 o[4];
  f32x16 negm;
  float m, l;
};

template <int N> DI void wait_vm() { asm volatile("s_waitcnt vmcnt(%0)" ::"i"(N) : "memory"); }

struct AttnCtx {
  const char* kbase;
  const char* vbase;
  unsigned koff[2], voff[2];
  unsigned ldsw;
  unsigned kaddr[8];
  unsigned vaddr[8];
};

#define SBAR() __builtin_amdgcn_sched_barrier(0)
constexpr float THR = 8.0f;
template <int SL> DI void issue_k(const AttnCtx& c, int t) {
  const char* kb = c.kbase + (size_t)t * 16384;
  unsigned l0 = SL * 16384 + c.ldsw;
  dma16(l0, c.koff[0], kb);
  dma16(l0 + 1024, c.koff[1], kb);
}
template <int SL> DI void issue_v(const AttnCtx& c, int t) {
  const char* vb = c.vbase + (size_t)t * 16384;
  unsigned l0 = 65536 + SL * 16384 + c.ldsw;
  dma16(l0, c.voff[0], vb);
  dma16(l0 + 1024, c.voff[1], vb);
}
DI float rowmax16(const f32x16& s) {
  float pm = s[0];
#pragma unroll
  for (int i = 1; i < 16; ++i) pm = fmaxf(pm, s[i]);
  auto rr = __builtin_amdgcn_permlane32_swap(__float_as_uint(pm), __float_as_uint(pm), false, false);
  return fmaxf(__uint_as_float(rr[0]), __uint_as_float(rr[1]));
}
template <int SL, int WAITN, bool ISSUEK, bool ISSUEV, bool NEXT> DI void step(const AttnCtx& c, AttnState& st, f32x16& sc, const bf16x8* qf, const char* lds, int t) {
  wait_vm<WAITN>();
  asm volatile("s_waitcnt lgkmcnt(0)" ::: "memory");
  __builtin_amdgcn_s_barrier();
  SBAR();
  const char* Ks = lds + ((SL + 1) & 3) * 16384;
  const char* Vs = lds + SL * 16384;
  bf16x8 kf[8];
  if (NEXT) {
#pragma unroll
    for (int i = 0; i < 8; ++i) kf[i] = *(const bf16x8*)(Ks + c.kaddr[i]);
  }
  SBAR();
  if (ISSUEK) issue_k<SL>(c, t + 4);
  if (ISSUEV) issue_v<(SL + 3) & 3>(c, t + 3);
  SBAR();
  f32x16 sn;
  if (NEXT) {
    asm("v_mfma_f32_32x32x16_bf16 %0, %1, %2, %3" : "=&v"(sn) : "v"(kf[0]), "v"(qf[0]), "v"(st.negm));
#pragma unroll
    for (int i = 1; i < 8; ++i) sn = mfma32(kf[i], qf[i], sn);
  }
  s16x4 vlo[4][2], vhi[4][2];
#pragma unroll
  for (int k = 0; k < 2; ++k)
#pragma unroll
    for (int d = 0; d < 4; ++d) {
      vlo[d][k] = tr_read(Vs + c.vaddr[2 * d] + k * 4096);
      vhi[d][k] = tr_read(Vs + c.vaddr[2 * d + 1] + k * 4096);
    }
  float ps = 0.f;
#pragma unroll
  for (int i = 0; i < 16; ++i) { sc[i] = __builtin_amdgcn_exp2f(sc[i]); ps += sc[i]; }
  st.l += ps;
  bf16x8 pf[2];
#pragma unroll
  for (int k = 0; k < 2; ++k) {
    u32x4 w = {cvtpk(sc[8 * k], sc[8 * k + 1]), cvtpk(sc[8 * k + 2], sc[8 * k + 3]), cvtpk(sc[8 * k + 4], sc[8 * k + 5]), cvtpk(sc[8 * k + 6], sc[8 * k + 7])};
    pf[k] = __builtin_bit_cast(bf16x8, w);
  }
  if (NEXT) {
#pragma unroll
    for (int i = 0; i < 8; ++i) {
      __builtin_amdgcn_sched_group_barrier(0x8, 1, 0);
      __builtin_amdgcn_sched_group_barrier(0x100, 2, 0);
      __builtin_amdgcn_sched_group_barrier(0x2, 5, 0);
    }
  }
  SBAR();
#pragma unroll
  for (int k = 0; k < 2; ++k) {
#pragma unroll
    for (int d = 0; d < 4; ++d) {
      bf16x8 vf = __builtin_shufflevector(vlo[d][k], vhi[d][k], 0, 1, 2, 3, 4, 5, 6, 7);
      st.o[d] = mfma32(vf, pf[k], st.o[d]);
    }
  }
  if (NEXT) {
    const float pm = rowmax16(sn);
    SBAR();
    if (__builtin_expect(__any(pm > THR), 0)) {
      const float dl = fmaxf(pm, 0.f);
      const float alpha = __builtin_amdgcn_exp2f(-dl);
      st.m += dl;
      st.l *= alpha;
#pragma unroll
      for (int i = 0; i < 16; ++i) { sn[i] -= dl; st.negm[i] = -st.m; }
#pragma unroll
      for (int d = 0; d < 4; ++d)
#pragma unroll
        for (int i = 0; i < 16; ++i) st.o[d][i] *= alpha;
    }
    sc = sn;
  }
  SBAR();
}

__global__ __launch_bounds__(512, 2) void attn_kernel(const u16* __restrict__ x, const u16* __restrict__ wq, const u16* __restrict__ kc,
                                                      const u16* __restrict__ vc, u16* __restrict__ og) {
  extern __shared__ __attribute__((aligned(16))) char lds[];
  const int bid = blockIdx.x;
  const int hkv = 2 * (bid & 7) + ((bid >> 3) & 1), b = bid >> 4;
  const int rot = (b * 9) >> 3;
  const int tid = threadIdx.x, lane = tid & 63, r = lane & 31, h = lane >> 5;
  const int wid = __builtin_amdgcn_readfirstlane(tid >> 6);
  const int kg = wid >> 2, g = (wid >> 1) & 1, pb = wid & 1;
  const int head = hkv * 2 + g;

  AttnCtx c;
  c.kbase = (const char*)(kc + (size_t)(b * HKV + hkv) * SK * DH);
  c.vbase = (const char*)(vc + (size_t)(b * HKV + hkv) * SK * DH);
  c.ldsw = wid * 8 * 256;
#pragma unroll
  for (int i = 0; i < 2; ++i) {
    const int row = 8 * wid + 4 * i + (lane >> 4), pos = lane & 15;
    c.koff[i] = row * 256 + ((pos ^ (row & 15)) << 4);
    c.voff[i] = row * 256 + ((pos ^ (((row & 3) << 2) | ((row >> 2) & 3))) << 4);
  }
  {
    const int krow = 32 * kg + r;
#pragma unroll
    for (int i = 0; i < 8; ++i) c.kaddr[i] = krow * 256 + (((2 * i + h) ^ (r & 15)) << 4);
    const int blk = (lane >> 4) & 1, q4 = (lane & 15) >> 2, p4 = lane & 3;
#pragma unroll
    for (int d = 0; d < 4; ++d)
#pragma unroll
      for (int t = 0; t < 2; ++t) {
        const int row = 32 * kg + 8 * t + 4 * h + q4;
        const int ch = 4 * d + 2 * blk + (p4 >> 1);
        const int sw = ((row & 3) << 2) | ((row >> 2) & 3);
        c.vaddr[2 * d + t] = 65536 + row * 256 + ((ch ^ sw) << 4) + 8 * (p4 & 1);
        asm volatile("" : "+v"(c.vaddr[2 * d + t]));
      }
  }
  const char* wqb = (const char*)(wq + (size_t)hkv * 256);
  const char* xb = (const char*)(x + (size_t)b * SQ * DM);
  constexpr unsigned XB0 = 131072;
  unsigned wsrc[2], xsrc;
#pragma unroll
  for (int i = 0; i < 2; ++i) {
    const int row = 4 * wid + 2 * i + (lane >> 5), pos = lane & 31;
    const int f = ((row & 3) << 2) | ((row >> 2) & 3);
    wsrc[i] = row * 8192 + ((pos ^ f) << 4);
  }
  {
    const int row = 16 * (wid & 3) + (lane >> 2), pos = lane & 3;
    xsrc = row * 1152 + ((pos ^ ((row >> 2) & 3)) << 4);
  }
  auto issue_chunk = [&](int chi, unsigned wbuf, unsigned xbuf) {
    int ch = chi + rot; if (ch >= 18) ch -= 18;
    const char* wsb = wqb + (size_t)ch * 32 * 8192;
#pragma unroll
    for (int i = 0; i < 2; ++i) dma16p(wbuf + wid * 2048 + i * 1024, wsrc[i], wsb);
    if (wid < 4) dma16p(xbuf + wid * 1024, xsrc, xb + ch * 64);
  };
  constexpr unsigned PS[8] = {6, 7, 0, 1, 4, 2, 5, 3};
#pragma unroll
  for (int ci = 0; ci < 8; ++ci) issue_chunk(ci, PS[ci] * 16384, XB0 + ci * 4096);
  unsigned waddr[2][2], xaddr[2];
  {
    const int blk = (lane >> 4) & 1, q4 = (lane & 15) >> 2, p4 = lane & 3;
#pragma unroll
    for (int j = 0; j < 2; ++j)
#pragma unroll
      for (int t = 0; t < 2; ++t) {
        const int row = 8 * h + 4 * t + q4;
        const int chn = 4 * (4 * g + 2 * kg + j) + 2 * blk + (p4 >> 1);
        const int f = ((row & 3) << 2) | ((row >> 2) & 3);
        waddr[j][t] = row * 512 + ((chn ^ f) << 4) + 8 * (p4 & 1);
      }
    const int row = 32 * pb + r;
#pragma unroll
    for (int s = 0; s < 2; ++s) xaddr[s] = row * 64 + (((2 * s + h) ^ ((row >> 2) & 3)) << 4);
  }
  f32x16 qa[2] = {};
  auto proj_chunk = [&](unsigned wbuf, unsigned xbuf) {
#pragma unroll
    for (int s = 0; s < 2; ++s) {
      const bf16x8 xf = *(const bf16x8*)(lds + xbuf + xaddr[s]);
#pragma unroll
      for (int j = 0; j < 2; ++j) {
        s16x4 lo = tr_read(lds + wbuf + s * 8192 + waddr[j][0]);
        s16x4 hi = tr_read(lds + wbuf + s * 8192 + waddr[j][1]);
        qa[j] = mfma32(__builtin_shufflevector(lo, hi, 0, 1, 2, 3, 4, 5, 6, 7), xf, qa[j]);
      }
    }
  };
#pragma unroll
  for (int ch = 0; ch < 18; ++ch) {
    {
      const int yc = ch == 0 ? 7 : ((ch + 6 <= 17 ? ch + 6 : 17) - ch);
      const int yt = ch > 11 ? ch - 11 : 0;
      if (wid < 4) asm volatile("s_waitcnt vmcnt(%0)" ::"i"(3 * yc + 2 * yt) : "memory");
      else asm volatile("s_waitcnt vmcnt(%0)" ::"i"(2 * yc + 2 * yt) : "memory");
    }
    asm volatile("s_waitcnt lgkmcnt(0)" ::: "memory");
    __builtin_amdgcn_s_barrier();
    if (ch >= 1 && ch <= 10) issue_chunk(ch + 7, PS[(ch + 7) & 7] * 16384, XB0 + ((ch + 7) & 7) * 4096);
    if (ch == 11) issue_k<0>(c, 0);
    if (ch == 12) issue_k<1>(c, 1);
    if (ch == 13) issue_v<0>(c, 0);
    if (ch == 14) issue_k<2>(c, 2);
    if (ch == 15) issue_v<1>(c, 1);
    if (ch == 16) issue_k<3>(c, 3);
    if (ch == 17) issue_v<2>(c, 2);
    proj_chunk(PS[ch & 7] * 16384, XB0 + (ch & 7) * 4096);
  }
  asm volatile("s_waitcnt lgkmcnt(0)" ::: "memory");
  __builtin_amdgcn_s_barrier();
  {
    char* qt = lds + 131072 + (g * 2 + pb) * 8192 + r * 256;
#pragma unroll
    for (int j = 0; j < 2; ++j)
#pragma unroll
      for (int a = 0; a < 4; ++a) {
        unsigned w0 = cvtpk(qa[j][4 * a], qa[j][4 * a + 1]), w1 = cvtpk(qa[j][4 * a + 2], qa[j][4 * a + 3]);
        w0 = cvtpk(__uint_as_float(w0 << 16) * SC, __uint_as_float(w0 & 0xffff0000u) * SC);
        w1 = cvtpk(__uint_as_float(w1 << 16) * SC, __uint_as_float(w1 & 0xffff0000u) * SC);
        u32x2 w = {w0, w1};
        *(u32x2*)(qt + (((8 * kg + 4 * j + a) ^ (r & 15)) << 4) + 8 * h) = w;
      }
  }
  wait_vm<12>();
  asm volatile("s_waitcnt lgkmcnt(0)" ::: "memory");
  __builtin_amdgcn_s_barrier();
  bf16x8 qf[8];
  {
    const char* qt = lds + 131072 + (g * 2 + pb) * 8192 + r * 256;
#pragma unroll
    for (int i = 0; i < 8; ++i) qf[i] = *(const bf16x8*)(qt + (((2 * i + h) ^ (r & 15)) << 4));
  }

  AttnState st;
#pragma unroll
  for (int d = 0; d < 4; ++d) st.o[d] = f32x16{};
  st.m = -1e30f; st.l = 0.f;

  f32x16 sc = {};
#pragma unroll
  for (int i = 0; i < 8; ++i) sc = mfma32(*(const bf16x8*)(lds + c.kaddr[i]), qf[i], sc);
  st.m = rowmax16(sc);
#pragma unroll
  for (int i = 0; i < 16; ++i) { sc[i] -= st.m; st.negm[i] = -st.m; }
  step<0, 8, true, true, true>(c, st, sc, qf, lds, 0);
  step<1, 8, true, true, true>(c, st, sc, qf, lds, 1);
  step<2, 8, true, true, true>(c, st, sc, qf, lds, 2);
  step<3, 8, true, true, true>(c, st, sc, qf, lds, 3);
#pragma unroll 1
  for (int t = 4; t < 12; t += 4) {
    step<0, 8, true, true, true>(c, st, sc, qf, lds, t);
    step<1, 8, true, true, true>(c, st, sc, qf, lds, t + 1);
    step<2, 8, true, true, true>(c, st, sc, qf, lds, t + 2);
    step<3, 8, true, true, true>(c, st, sc, qf, lds, t + 3);
  }
  step<0, 8, false, true, true>(c, st, sc, qf, lds, 12);
  step<1, 6, false, false, true>(c, st, sc, qf, lds, 13);
  step<2, 2, false, false, true>(c, st, sc, qf, lds, 14);
  step<3, 0, false, false, false>(c, st, sc, qf, lds, 15);

  float lt = st.l + __shfl_xor(st.l, 32);
  __syncthreads();
  char* xw = lds + wid * 8704;
  char* xp = lds + (wid ^ 4) * 8704;
  {
#pragma unroll
    for (int dd = 0; dd < 2; ++dd) {
      const f32x16 v = kg ? st.o[dd] : st.o[2 + dd];
#pragma unroll
      for (int a = 0; a < 4; ++a) {
        f32x4 w = {v[4 * a], v[4 * a + 1], v[4 * a + 2], v[4 * a + 3]};
        *(f32x4*)(xw + ((dd * 4 + a) * 64 + lane) * 16) = w;
      }
    }
    f32x2 ml = {st.m, lt};
    *(f32x2*)(xw + 8192 + lane * 8) = ml;
  }
  __syncthreads();
  f32x2 pml = *(const f32x2*)(xp + 8192 + lane * 8);
  const float mm = fmaxf(st.m, pml[0]);
  const float fa = __builtin_amdgcn_exp2f(st.m - mm), fb = __builtin_amdgcn_exp2f(pml[0] - mm);
  const float inv = 1.0f / (lt * fa + pml[1] * fb);
  const float wa = fa * inv, wb = fb * inv;
  char* stg = lds + 8 * 8704 + wid * 4608;
#pragma unroll
  for (int dd = 0; dd < 2; ++dd) {
    const f32x16 own = kg ? st.o[2 + dd] : st.o[dd];
#pragma unroll
    for (int a = 0; a < 4; ++a) {
      f32x4 pv = *(const f32x4*)(xp + ((dd * 4 + a) * 64 + lane) * 16);
      float o0 = own[4 * a] * wa + pv[0] * wb, o1 = own[4 * a + 1] * wa + pv[1] * wb;
      float o2 = own[4 * a + 2] * wa + pv[2] * wb, o3 = own[4 * a + 3] * wa + pv[3] * wb;
      u32x2 w = {cvtpk(o0, o1), cvtpk(o2, o3)};
      *(u32x2*)(stg + r * 144 + (32 * dd + 8 * a + 4 * h) * 2) = w;
    }
  }
  asm volatile("s_waitcnt lgkmcnt(0)" ::: "memory");
  __builtin_amdgcn_wave_barrier();
  u16* obase = og + (size_t)(b * SQ + 32 * pb) * QW + head * DH + kg * 64;
#pragma unroll
  for (int i = 0; i < 4; ++i) {
    const int row = i * 8 + (lane >> 3), ch = lane & 7;
    u32x4 w = *(const u32x4*)(stg + row * 144 + ch * 16);
    *(u32x4*)(obase + (size_t)row * QW + ch * 8) = w;
  }
}

DI f32x4 mfma16(bf16x8 a, bf16x8 b, f32x4 c) { return __builtin_amdgcn_mfma_f32_16x16x32_bf16(a, b, c, 0, 0, 0); }
constexpr int OP_GSTRIDE = 2432;
constexpr int OP_LDS = 64 * OP_GSTRIDE;
__global__ __launch_bounds__(512, 2) void oproj_kernel(const u16* __restrict__ og, const u16* __restrict__ wo, float* __restrict__ part) {
  extern __shared__ __attribute__((aligned(16))) char lds[];
  const int bid = blockIdx.x;
  const int ks = bid & 7, nt = (bid >> 3) & 3, mt = bid >> 5;
  const int tid = threadIdx.x, lane = tid & 63;
  const int wid = __builtin_amdgcn_readfirstlane(tid >> 6);
  const int mq = wid & 3, kh = wid >> 2;
  const int l15 = lane & 15, g4 = lane >> 4;
  const int rot = (mt + 2 * nt) & 7;
  const char* ob = (const char*)(og + (size_t)(128 * mt + 32 * mq) * QW + 512 * ks + 32 * kh);
  const unsigned ooff0 = l15 * 8192 + g4 * 16, ooff1 = ooff0 + 16 * 8192;
  const char* wb = (const char*)(wo + (size_t)(512 * ks) * DM + 144 * nt);
  auto wsrc_of = [&](int p) -> unsigned {
    const int idx = p * 64 + lane;
    const int grp = idx / 152, rem = idx - grp * 152;
    const int rr = rem / 18, ch = rem - rr * 18;
    return rem < 144 ? (unsigned)((8 * grp + rr) * 1152 + ch * 16) : 0u;
  };
  bf16x8 of[2][8];
  auto load_o = [&](int s) {
    const char* p = ob + ((s + rot) & 7) * 128;
    asm volatile("global_load_dwordx4 %0, %1, %2" : "=v"(of[0][s]) : "v"(ooff0), "s"(p) : "memory");
    asm volatile("global_load_dwordx4 %0, %1, %2" : "=v"(of[1][s]) : "v"(ooff1), "s"(p) : "memory");
  };
  auto dma_stage = [&](int st) {
#pragma unroll
    for (int i = 0; i < 10; ++i) {
      const int j = wid + 8 * i;
      if (j < 76) {
        const int cc = j / 19, pin = j - cc * 19;
        const int p = ((4 * st + cc + rot) & 7) * 19 + pin;
        dma16(p * 1024, wsrc_of(p), wb);
      }
    }
  };
#pragma unroll
  for (int s = 0; s < 4; ++s) load_o(s);
  dma_stage(0);
#pragma unroll
  for (int s = 4; s < 8; ++s) load_o(s);
  dma_stage(1);

  f32x4 acc[2][9];
#pragma unroll
  for (int i = 0; i < 2; ++i)
#pragma unroll
    for (int c = 0; c < 9; ++c) acc[i][c] = f32x4{0.f, 0.f, 0.f, 0.f};
  const int q4 = (lane & 15) >> 2, p4 = lane & 3;
  const unsigned abase = (unsigned)((4 * kh + g4) * OP_GSTRIDE + q4 * 288 + p4 * 8);
  auto compute_half = [&](int hf) {
#pragma unroll
    for (int s = 4 * hf; s < 4 * hf + 4; ++s) {
      const char* ap = lds + abase + ((s + rot) & 7) * 8 * OP_GSTRIDE;
#pragma unroll
      for (int c = 0; c < 9; ++c) {
        s16x4 lo = tr_read(ap + c * 32), hi = tr_read(ap + c * 32 + 4 * 288);
        bf16x8 af = __builtin_shufflevector(lo, hi, 0, 1, 2, 3, 4, 5, 6, 7);
        acc[0][c] = mfma16(af, of[0][s], acc[0][c]);
        acc[1][c] = mfma16(af, of[1][s], acc[1][c]);
      }
    }
  };
  asm volatile("s_waitcnt vmcnt(17)" : "+v"(of[0][0]), "+v"(of[0][1]), "+v"(of[0][2]), "+v"(of[0][3]), "+v"(of[1][0]), "+v"(of[1][1]), "+v"(of[1][2]), "+v"(of[1][3]) :: "memory");
  __builtin_amdgcn_s_barrier();
  compute_half(0);
  asm volatile("s_waitcnt vmcnt(0)" : "+v"(of[0][4]), "+v"(of[0][5]), "+v"(of[0][6]), "+v"(of[0][7]), "+v"(of[1][4]), "+v"(of[1][5]), "+v"(of[1][6]), "+v"(of[1][7]) :: "memory");
  __builtin_amdgcn_s_barrier();
  compute_half(1);
  __syncthreads();
  {
    char* xs = lds + wid * 9216 + lane * 16;
#pragma unroll
    for (int c = 0; c < 9; ++c) *(f32x4*)(xs + c * 1024) = kh ? acc[0][c] : acc[1][c];
  }
  __syncthreads();
  {
    const char* xr = lds + (wid ^ 4) * 9216 + lane * 16;
    float* pp = part + ((size_t)ks * MROWS + 128 * mt + 32 * mq + 16 * kh + l15) * DM + 144 * nt + 4 * g4;
#pragma unroll
    for (int c = 0; c < 9; ++c) {
      f32x4 v = *(const f32x4*)(xr + c * 1024);
      f32x4 o = kh ? acc[1][c] : acc[0][c];
      *(f32x4*)(pp + 16 * c) = v + o;
    }
  }
}

__global__ __launch_bounds__(256) void reduce_kernel(const float* __restrict__ part, u16* __restrict__ out) {
  const int i = blockIdx.x * 256 + threadIdx.x;
  f32x4 s = *(const f32x4*)(part + (size_t)i * 4);
#pragma unroll
  for (int k = 1; k < 8; ++k) s += *(const f32x4*)(part + (size_t)k * MROWS * DM + (size_t)i * 4);
  u32x2 w = {cvtpk(s[0], s[1]), cvtpk(s[2], s[3])};
  *(u32x2*)(out + (size_t)i * 4) = w;
}

extern "C" void kernel_launch(void* const* d_in, const int* in_sizes, int n_in, void* d_out, int out_size, void* d_ws, size_t ws_size,
                              hipStream_t stream) {
  const u16* x = (const u16*)d_in[0];
  const u16* kc = (const u16*)d_in[1];
  const u16* vc = (const u16*)d_in[2];
  const u16* wq = (const u16*)d_in[3];
  const u16* wo = (const u16*)d_in[4];
  u16* out = (u16*)d_out;
  u16* qws = (u16*)d_ws;
  u16* ows = qws + (size_t)MROWS * QW;
  hipFuncSetAttribute((const void*)attn_kernel, hipFuncAttributeMaxDynamicSharedMemorySize, ATT_LDS);
  attn_kernel<<<256, 512, ATT_LDS, stream>>>(x, wq, kc, vc, ows);
  float* part = (float*)((char*)d_ws + (32 << 20));
  hipFuncSetAttribute((const void*)oproj_kernel, hipFuncAttributeMaxDynamicSharedMemorySize, OP_LDS);
  oproj_kernel<<<256, 512, OP_LDS, stream>>>(ows, wo, part);
  reduce_kernel<<<MROWS * DM / 4 / 256, 256, 0, stream>>>(part, out);
}
```

```cpp
#define MAPPING 0
#include <hip/hip_runtime.h>
#include <hip/hip_bf16.h>
#include <stdint.h>

typedef __attribute__((ext_vector_type(8))) short bf16x8;
typedef __attribute__((ext_vector_type(4))) short s16x4;
typedef __attribute__((ext_vector_type(16))) float f32x16;
typedef __attribute__((ext_vector_type(4))) float f32x4;
typedef __attribute__((ext_vector_type(2))) float f32x2;
typedef __attribute__((ext_vector_type(4))) unsigned u32x4;
typedef __attribute__((ext_vector_type(2))) unsigned u32x2;
typedef __attribute__((ext_vector_type(2))) __bf16 bf16x2_t;
typedef unsigned short u16;
typedef __attribute__((address_space(3))) s16x4* lds_s16x4_p;

#define DI __device__ __forceinline__

constexpr int NB = 16, SQ = 64, DM = 576, HQ = 32, HKV = 16, SK = 1024, DH = 128;
constexpr int MROWS = NB * SQ;
constexpr int QW = HQ * DH;

DI unsigned cvtpk(float lo, float hi) {
  f32x2 v = {lo, hi};
  bf16x2_t b = __builtin_convertvector(v, bf16x2_t);
  return __builtin_bit_cast(unsigned, b);
}
DI s16x4 tr_read(const char* p) {
  return __builtin_amdgcn_ds_read_tr16_b64_v4i16((lds_s16x4_p)(p));
}
DI f32x16 mfma32(bf16x8 a, bf16x8 b, f32x16 c) { return __builtin_amdgcn_mfma_f32_32x32x16_bf16(a, b, c, 0, 0, 0); }

__global__ __launch_bounds__(256) void gemm_simple(const u16* __restrict__ A, const u16* __restrict__ Bm, u16* __restrict__ C,
                                                   int N, int K) {
  __shared__ __attribute__((aligned(16))) char lds[64 * 80 + 32 * 192];
  char* As = lds;
  char* Bs = lds + 64 * 80;
  const int tid = threadIdx.x, wid = tid >> 6, lane = tid & 63, r = lane & 31, h = lane >> 5;
  const int m0 = blockIdx.y * 64, n0 = blockIdx.x * 64;
  const int wm = wid >> 1, wn = wid & 1;
  f32x16 acc = {};
  const int arow = tid >> 2, ach = tid & 3;
  const int brow = tid >> 3, bch = tid & 7;
  const int blk = (lane >> 4) & 1, q4 = (lane & 15) >> 2, p4 = lane & 3;
  for (int k0 = 0; k0 < K; k0 += 32) {
    bf16x8 av = *(const bf16x8*)(A + (size_t)(m0 + arow) * K + k0 + ach * 8);
    bf16x8 bv = *(const bf16x8*)(Bm + (size_t)(k0 + brow) * N + n0 + bch * 8);
    __syncthreads();
    *(bf16x8*)(As + arow * 80 + ach * 16) = av;
    *(bf16x8*)(Bs + brow * 192 + bch * 16) = bv;
    __syncthreads();
#pragma unroll
    for (int s = 0; s < 2; ++s) {
      bf16x8 xa = *(const bf16x8*)(As + (wm * 32 + r) * 80 + s * 32 + h * 16);
      s16x4 lo = tr_read(Bs + (16 * s + 8 * h + q4) * 192 + (wn * 32 + 16 * blk + 4 * p4) * 2);
      s16x4 hi = tr_read(Bs + (16 * s + 8 * h + 4 + q4) * 192 + (wn * 32 + 16 * blk + 4 * p4) * 2);
      bf16x8 wb = __builtin_shufflevector(lo, hi, 0, 1, 2, 3, 4, 5, 6, 7);
      acc = mfma32(wb, xa, acc);
    }
  }
  u16* crow = C + (size_t)(m0 + wm * 32 + r) * N + n0 + wn * 32;
#pragma unroll
  for (int a = 0; a < 4; ++a) {
    u32x2 w = {cvtpk(acc[4 * a], acc[4 * a + 1]), cvtpk(acc[4 * a + 2], acc[4 * a + 3])};
    *(u32x2*)(crow + 8 * a + 4 * h) = w;
  }
}

constexpr int NS = 4;
constexpr int SLOT = 32768;
constexpr int ATT_LDS = 163840;
constexpr float SC = 0.08838834764831845f * 1.4426950408889634f;

DI void dma16(unsigned ldsaddr, unsigned voff, const void* sbase) {
  unsigned keep;
  asm volatile("s_mov_b32 %0, m0\n\ts_mov_b32 m0, %1\n\ts_nop 0\n\tglobal_load_lds_dwordx4 %2, %3 nt\n\ts_mov_b32 m0, %0"
               : "=&s"(keep) : "s"(ldsaddr), "v"(voff), "s"(sbase) : "memory");
}

DI void dma16p(unsigned ldsaddr, unsigned voff, const void* sbase) {
  unsigned keep;
  asm volatile("s_mov_b32 %0, m0\n\ts_mov_b32 m0, %1\n\ts_nop 0\n\tglobal_load_lds_dwordx4 %2, %3\n\ts_mov_b32 m0, %0"
               : "=&s"(keep) : "s"(ldsaddr), "v"(voff), "s"(sbase) : "memory");
}

struct AttnState {
  f32x16 o[4];
  f32x16 negm;
  float m, l;
};

template <int N> DI void wait_vm() { asm volatile("s_waitcnt vmcnt(%0)" ::"i"(N) : "memory"); }

struct AttnCtx {
  const char* kbase;
  const char* vbase;
  unsigned koff[2], voff[2];
  unsigned ldsw;
  unsigned kaddr[8];
  unsigned vaddr[8];
};

#define SBAR() __builtin_amdgcn_sched_barrier(0)
constexpr float THR = 8.0f;
template <int SL> DI void issue_k(const AttnCtx& c, int t) {
  const char* kb = c.kbase + (size_t)t * 16384;
  unsigned l0 = SL * 16384 + c.ldsw;
  dma16(l0, c.koff[0], kb);
  dma16(l0 + 1024, c.koff[1], kb);
}
template <int SL> DI void issue_v(const AttnCtx& c, int t) {
  const char* vb = c.vbase + (size_t)t * 16384;
  unsigned l0 = 65536 + SL * 16384 + c.ldsw;
  dma16(l0, c.voff[0], vb);
  dma16(l0 + 1024, c.voff[1], vb);
}
DI float rowmax16(const f32x16& s) {
  float pm = s[0];
#pragma unroll
  for (int i = 1; i < 16; ++i) pm = fmaxf(pm, s[i]);
  auto rr = __builtin_amdgcn_permlane32_swap(__float_as_uint(pm), __float_as_uint(pm), false, false);
  return fmaxf(__uint_as_float(rr[0]), __uint_as_float(rr[1]));
}
template <int SL, int WAITN, bool ISSUEK, bool ISSUEV, bool NEXT> DI void step(const AttnCtx& c, AttnState& st, f32x16& sc, const bf16x8* qf, const char* lds, int t) {
  wait_vm<WAITN>();
  asm volatile("s_waitcnt lgkmcnt(0)" ::: "memory");
  __builtin_amdgcn_s_barrier();
  SBAR();
  const char* Ks = lds + ((SL + 1) & 3) * 16384;
  const char* Vs = lds + SL * 16384;
  bf16x8 kf[8];
  if (NEXT) {
#pragma unroll
    for (int i = 0; i < 8; ++i) kf[i] = *(const bf16x8*)(Ks + c.kaddr[i]);
  }
  SBAR();
  if (ISSUEK) issue_k<SL>(c, t + 4);
  if (ISSUEV) issue_v<(SL + 3) & 3>(c, t + 3);
  SBAR();
  f32x16 sn;
  if (NEXT) {
    asm("v_mfma_f32_32x32x16_bf16 %0, %1, %2, %3" : "=&v"(sn) : "v"(kf[0]), "v"(qf[0]), "v"(st.negm));
#pragma unroll
    for (int i = 1; i < 8; ++i) sn = mfma32(kf[i], qf[i], sn);
  }
  s16x4 vlo[4][2], vhi[4][2];
#pragma unroll
  for (int k = 0; k < 2; ++k)
#pragma unroll
    for (int d = 0; d < 4; ++d) {
      vlo[d][k] = tr_read(Vs + c.vaddr[2 * d] + k * 4096);
      vhi[d][k] = tr_read(Vs + c.vaddr[2 * d + 1] + k * 4096);
    }
  float ps = 0.f;
#pragma unroll
  for (int i = 0; i < 16; ++i) { sc[i] = __builtin_amdgcn_exp2f(sc[i]); ps += sc[i]; }
  st.l += ps;
  bf16x8 pf[2];
#pragma unroll
  for (int k = 0; k < 2; ++k) {
    u32x4 w = {cvtpk(sc[8 * k], sc[8 * k + 1]), cvtpk(sc[8 * k + 2], sc[8 * k + 3]), cvtpk(sc[8 * k + 4], sc[8 * k + 5]), cvtpk(sc[8 * k + 6], sc[8 * k + 7])};
    pf[k] = __builtin_bit_cast(bf16x8, w);
  }
  if (NEXT) {
#pragma unroll
    for (int i = 0; i < 8; ++i) {
      __builtin_amdgcn_sched_group_barrier(0x8, 1, 0);
      __builtin_amdgcn_sched_group_barrier(0x100, 2, 0);
      __builtin_amdgcn_sched_group_barrier(0x2, 5, 0);
    }
  }
  SBAR();
#pragma unroll
  for (int k = 0; k < 2; ++k) {
#pragma unroll
    for (int d = 0; d < 4; ++d) {
      bf16x8 vf = __builtin_shufflevector(vlo[d][k], vhi[d][k], 0, 1, 2, 3, 4, 5, 6, 7);
      st.o[d] = mfma32(vf, pf[k], st.o[d]);
    }
  }
  if (NEXT) {
    const float pm = rowmax16(sn);
    SBAR();
    if (__builtin_expect(__any(pm > THR), 0)) {
      const float dl = fmaxf(pm, 0.f);
      const float alpha = __builtin_amdgcn_exp2f(-dl);
      st.m += dl;
      st.l *= alpha;
#pragma unroll
      for (int i = 0; i < 16; ++i) { sn[i] -= dl; st.negm[i] = -st.m; }
#pragma unroll
      for (int d = 0; d < 4; ++d)
#pragma unroll
        for (int i = 0; i < 16; ++i) st.o[d][i] *= alpha;
    }
    sc = sn;
  }
  SBAR();
}

__global__ __launch_bounds__(512, 2) void attn_kernel(const u16* __restrict__ x, const u16* __restrict__ wq, const u16* __restrict__ kc,
                                                      const u16* __restrict__ vc, u16* __restrict__ og) {
  extern __shared__ __attribute__((aligned(16))) char lds[];
  const int bid = blockIdx.x;
  const int hkv = 2 * (bid & 7) + ((bid >> 3) & 1), b = bid >> 4;
  const int rot = (b * 9) >> 3;
  const int tid = threadIdx.x, lane = tid & 63, r = lane & 31, h = lane >> 5;
  const int wid = __builtin_amdgcn_readfirstlane(tid >> 6);
  const int kg = wid >> 2, g = (wid >> 1) & 1, pb = wid & 1;
  const int head = hkv * 2 + g;

  AttnCtx c;
  c.kbase = (const char*)(kc + (size_t)(b * HKV + hkv) * SK * DH);
  c.vbase = (const char*)(vc + (size_t)(b * HKV + hkv) * SK * DH);
  c.ldsw = wid * 8 * 256;
#pragma unroll
  for (int i = 0; i < 2; ++i) {
    const int row = 8 * wid + 4 * i + (lane >> 4), pos = lane & 15;
    c.koff[i] = row * 256 + ((pos ^ (row & 15)) << 4);
    c.voff[i] = row * 256 + ((pos ^ (((row & 3) << 2) | ((row >> 2) & 3))) << 4);
  }
  {
    const int krow = 32 * kg + r;
#pragma unroll
    for (int i = 0; i < 8; ++i) c.kaddr[i] = krow * 256 + (((2 * i + h) ^ (r & 15)) << 4);
    const int blk = (lane >> 4) & 1, q4 = (lane & 15) >> 2, p4 = lane & 3;
#pragma unroll
    for (int d = 0; d < 4; ++d)
#pragma unroll
      for (int t = 0; t < 2; ++t) {
        const int row = 32 * kg + 8 * t + 4 * h + q4;
        const int ch = 4 * d + 2 * blk + (p4 >> 1);
        const int sw = ((row & 3) << 2) | ((row >> 2) & 3);
        c.vaddr[2 * d + t] = 65536 + row * 256 + ((ch ^ sw) << 4) + 8 * (p4 & 1);
        asm volatile("" : "+v"(c.vaddr[2 * d + t]));
      }
  }
  const char* wqb = (const char*)(wq + (size_t)hkv * 256);
  const char* xb = (const char*)(x + (size_t)b * SQ * DM);
  constexpr unsigned XB0 = 131072;
  unsigned wsrc[2], xsrc;
#pragma unroll
  for (int i = 0; i < 2; ++i) {
    const int row = 4 * wid + 2 * i + (lane >> 5), pos = lane & 31;
    const int f = ((row & 3) << 2) | ((row >> 2) & 3);
    wsrc[i] = row * 8192 + ((pos ^ f) << 4);
  }
  {
    const int row = 16 * (wid & 3) + (lane >> 2), pos = lane & 3;
    xsrc = row * 1152 + ((pos ^ ((row >> 2) & 3)) << 4);
  }
  auto issue_chunk = [&](int chi, unsigned wbuf, unsigned xbuf) {
    int ch = chi + rot; if (ch >= 18) ch -= 18;
    const char* wsb = wqb + (size_t)ch * 32 * 8192;
#pragma unroll
    for (int i = 0; i < 2; ++i) dma16p(wbuf + wid * 2048 + i * 1024, wsrc[i], wsb);
    if (wid < 4) dma16p(xbuf + wid * 1024, xsrc, xb + ch * 64);
  };
  constexpr unsigned PS[8] = {6, 7, 0, 1, 4, 2, 5, 3};
#pragma unroll
  for (int ci = 0; ci < 8; ++ci) issue_chunk(ci, PS[ci] * 16384, XB0 + ci * 4096);
  unsigned waddr[2][2], xaddr[2];
  {
    const int blk = (lane >> 4) & 1, q4 = (lane & 15) >> 2, p4 = lane & 3;
#pragma unroll
    for (int j = 0; j < 2; ++j)
#pragma unroll
      for (int t = 0; t < 2; ++t) {
        const int row = 8 * h + 4 * t + q4;
        const int chn = 4 * (4 * g + 2 * kg + j) + 2 * blk + (p4 >> 1);
        const int f = ((row & 3) << 2) | ((row >> 2) & 3);
        waddr[j][t] = row * 512 + ((chn ^ f) << 4) + 8 * (p4 & 1);
      }
    const int row = 32 * pb + r;
#pragma unroll
    for (int s = 0; s < 2; ++s) xaddr[s] = row * 64 + (((2 * s + h) ^ ((row >> 2) & 3)) << 4);
  }
  f32x16 qa[2] = {};
  auto proj_chunk = [&](unsigned wbuf, unsigned xbuf) {
#pragma unroll
    for (int s = 0; s < 2; ++s) {
      const bf16x8 xf = *(const bf16x8*)(lds + xbuf + xaddr[s]);
#pragma unroll
      for (int j = 0; j < 2; ++j) {
        s16x4 lo = tr_read(lds + wbuf + s * 8192 + waddr[j][0]);
        s16x4 hi = tr_read(lds + wbuf + s * 8192 + waddr[j][1]);
        qa[j] = mfma32(__builtin_shufflevector(lo, hi, 0, 1, 2, 3, 4, 5, 6, 7), xf, qa[j]);
      }
    }
  };
#pragma unroll
  for (int ch = 0; ch < 18; ++ch) {
    {
      const int yc = ch == 0 ? 7 : ((ch + 6 <= 17 ? ch + 6 : 17) - ch);
      const int yt = ch > 11 ? ch - 11 : 0;
      if (wid < 4) asm volatile("s_waitcnt vmcnt(%0)" ::"i"(3 * yc + 2 * yt) : "memory");
      else asm volatile("s_waitcnt vmcnt(%0)" ::"i"(2 * yc + 2 * yt) : "memory");
    }
    asm volatile("s_waitcnt lgkmcnt(0)" ::: "memory");
    __builtin_amdgcn_s_barrier();
    if (ch >= 1 && ch <= 10) issue_chunk(ch + 7, PS[(ch + 7) & 7] * 16384, XB0 + ((ch + 7) & 7) * 4096);
    if (ch == 11) issue_k<0>(c, 0);
    if (ch == 12) issue_k<1>(c, 1);
    if (ch == 13) issue_v<0>(c, 0);
    if (ch == 14) issue_k<2>(c, 2);
    if (ch == 15) issue_v<1>(c, 1);
    if (ch == 16) issue_k<3>(c, 3);
    if (ch == 17) issue_v<2>(c, 2);
    proj_chunk(PS[ch & 7] * 16384, XB0 + (ch & 7) * 4096);
  }
  asm volatile("s_waitcnt lgkmcnt(0)" ::: "memory");
  __builtin_amdgcn_s_barrier();
  {
    char* qt = lds + 131072 + (g * 2 + pb) * 8192 + r * 256;
#pragma unroll
    for (int j = 0; j < 2; ++j)
#pragma unroll
      for (int a = 0; a < 4; ++a) {
        unsigned w0 = cvtpk(qa[j][4 * a], qa[j][4 * a + 1]), w1 = cvtpk(qa[j][4 * a + 2], qa[j][4 * a + 3]);
        w0 = cvtpk(__uint_as_float(w0 << 16) * SC, __uint_as_float(w0 & 0xffff0000u) * SC);
        w1 = cvtpk(__uint_as_float(w1 << 16) * SC, __uint_as_float(w1 & 0xffff0000u) * SC);
        u32x2 w = {w0, w1};
        *(u32x2*)(qt + (((8 * kg + 4 * j + a) ^ (r & 15)) << 4) + 8 * h) = w;
      }
  }
  wait_vm<12>();
  asm volatile("s_waitcnt lgkmcnt(0)" ::: "memory");
  __builtin_amdgcn_s_barrier();
  bf16x8 qf[8];
  {
    const char* qt = lds + 131072 + (g * 2 + pb) * 8192 + r * 256;
#pragma unroll
    for (int i = 0; i < 8; ++i) qf[i] = *(const bf16x8*)(qt + (((2 * i + h) ^ (r & 15)) << 4));
  }

  AttnState st;
#pragma unroll
  for (int d = 0; d < 4; ++d) st.o[d] = f32x16{};
  st.m = -1e30f; st.l = 0.f;

  f32x16 sc = {};
#pragma unroll
  for (int i = 0; i < 8; ++i) sc = mfma32(*(const bf16x8*)(lds + c.kaddr[i]), qf[i], sc);
  st.m = rowmax16(sc);
#pragma unroll
  for (int i = 0; i < 16; ++i) { sc[i] -= st.m; st.negm[i] = -st.m; }
  step<0, 8, true, true, true>(c, st, sc, qf, lds, 0);
  step<1, 8, true, true, true>(c, st, sc, qf, lds, 1);
  step<2, 8, true, true, true>(c, st, sc, qf, lds, 2);
  step<3, 8, true, true, true>(c, st, sc, qf, lds, 3);
#pragma unroll 1
  for (int t = 4; t < 12; t += 4) {
    step<0, 8, true, true, true>(c, st, sc, qf, lds, t);
    step<1, 8, true, true, true>(c, st, sc, qf, lds, t + 1);
    step<2, 8, true, true, true>(c, st, sc, qf, lds, t + 2);
    step<3, 8, true, true, true>(c, st, sc, qf, lds, t + 3);
  }
  step<0, 8, false, true, true>(c, st, sc, qf, lds, 12);
  step<1, 6, false, false, true>(c, st, sc, qf, lds, 13);
  step<2, 2, false, false, true>(c, st, sc, qf, lds, 14);
  step<3, 0, false, false, false>(c, st, sc, qf, lds, 15);

  float lt = st.l + __shfl_xor(st.l, 32);
  __syncthreads();
  char* xw = lds + wid * 8704;
  char* xp = lds + (wid ^ 4) * 8704;
  {
#pragma unroll
    for (int dd = 0; dd < 2; ++dd) {
      const f32x16 v = kg ? st.o[dd] : st.o[2 + dd];
#pragma unroll
      for (int a = 0; a < 4; ++a) {
        f32x4 w = {v[4 * a], v[4 * a + 1], v[4 * a + 2], v[4 * a + 3]};
        *(f32x4*)(xw + ((dd * 4 + a) * 64 + lane) * 16) = w;
      }
    }
    f32x2 ml = {st.m, lt};
    *(f32x2*)(xw + 8192 + lane * 8) = ml;
  }
  __syncthreads();
  f32x2 pml = *(const f32x2*)(xp + 8192 + lane * 8);
  const float mm = fmaxf(st.m, pml[0]);
  const float fa = __builtin_amdgcn_exp2f(st.m - mm), fb = __builtin_amdgcn_exp2f(pml[0] - mm);
  const float inv = 1.0f / (lt * fa + pml[1] * fb);
  const float wa = fa * inv, wb = fb * inv;
  char* stg = lds + 8 * 8704 + wid * 4608;
#pragma unroll
  for (int dd = 0; dd < 2; ++dd) {
    const f32x16 own = kg ? st.o[2 + dd] : st.o[dd];
#pragma unroll
    for (int a = 0; a < 4; ++a) {
      f32x4 pv = *(const f32x4*)(xp + ((dd * 4 + a) * 64 + lane) * 16);
      float o0 = own[4 * a] * wa + pv[0] * wb, o1 = own[4 * a + 1] * wa + pv[1] * wb;
      float o2 = own[4 * a + 2] * wa + pv[2] * wb, o3 = own[4 * a + 3] * wa + pv[3] * wb;
      u32x2 w = {cvtpk(o0, o1), cvtpk(o2, o3)};
      *(u32x2*)(stg + r * 144 + (32 * dd + 8 * a + 4 * h) * 2) = w;
    }
  }
  asm volatile("s_waitcnt lgkmcnt(0)" ::: "memory");
  __builtin_amdgcn_wave_barrier();
  {
    const int col0 = head * DH + kg * 64;
    const int ksl = col0 >> 9, kst = (col0 & 511) >> 5;
    const int mt0 = (b * SQ + 32 * pb) >> 4;
#pragma unroll
    for (int i = 0; i < 4; ++i) {
      const int mi = i >> 1, ki = i & 1;
      u32x4 w = *(const u32x4*)(stg + (16 * mi + (lane & 15)) * 144 + ki * 64 + (lane >> 4) * 16);
      char* dst = (char*)og + ((size_t)((ksl * 64 + mt0 + mi) * 16 + kst + ki) << 10) + lane * 16;
      *(u32x4*)dst = w;
    }
  }
}

DI f32x4 mfma16(bf16x8 a, bf16x8 b, f32x4 c) { return __builtin_amdgcn_mfma_f32_16x16x32_bf16(a, b, c, 0, 0, 0); }
constexpr int OP_GSTRIDE = 2432;
constexpr int OP_LDS = 64 * OP_GSTRIDE;
__global__ __launch_bounds__(512, 2) void oproj_kernel(const u16* __restrict__ og, const u16* __restrict__ wo, float* __restrict__ part) {
  extern __shared__ __attribute__((aligned(16))) char lds[];
  const int bid = blockIdx.x;
  const int ks = bid & 7, nt = (bid >> 3) & 3, mt = bid >> 5;
  const int tid = threadIdx.x, lane = tid & 63;
  const int wid = __builtin_amdgcn_readfirstlane(tid >> 6);
  const int mq = wid & 3, kh = wid >> 2;
  const int l15 = lane & 15, g4 = lane >> 4;
  const int rot = (mt + 2 * nt) & 7;
  const char* ob = (const char*)og + ((size_t)((ks * 64 + 8 * mt + 2 * mq) * 16 + kh) << 10);
  const unsigned ooff0 = lane * 16, ooff1 = ooff0 + 16384;
  const char* wb = (const char*)(wo + (size_t)(512 * ks) * DM + 144 * nt);
  auto wsrc_of = [&](int p) -> unsigned {
    const int idx = p * 64 + lane;
    const int grp = idx / 152, rem = idx - grp * 152;
    const int rr = rem / 18, ch = rem - rr * 18;
    return rem < 144 ? (unsigned)((8 * grp + rr) * 1152 + ch * 16) : 0u;
  };
  bf16x8 of[2][8];
  auto load_o = [&](int s) {
    const char* p = ob + ((s + rot) & 7) * 2048;
    asm volatile("global_load_dwordx4 %0, %1, %2" : "=v"(of[0][s]) : "v"(ooff0), "s"(p) : "memory");
    asm volatile("global_load_dwordx4 %0, %1, %2" : "=v"(of[1][s]) : "v"(ooff1), "s"(p) : "memory");
  };
  auto dma_stage = [&](int st) {
#pragma unroll
    for (int i = 0; i < 10; ++i) {
      const int j = wid + 8 * i;
      if (j < 76) {
        const int cc = j / 19, pin = j - cc * 19;
        const int p = ((4 * st + cc + rot) & 7) * 19 + pin;
        dma16(p * 1024, wsrc_of(p), wb);
      }
    }
  };
#pragma unroll
  for (int s = 0; s < 4; ++s) load_o(s);
  dma_stage(0);
#pragma unroll
  for (int s = 4; s < 8; ++s) load_o(s);
  dma_stage(1);

  f32x4 acc[2][9];
#pragma unroll
  for (int i = 0; i < 2; ++i)
#pragma unroll
    for (int c = 0; c < 9; ++c) acc[i][c] = f32x4{0.f, 0.f, 0.f, 0.f};
  const int q4 = (lane & 15) >> 2, p4 = lane & 3;
  const unsigned abase = (unsigned)((4 * kh + g4) * OP_GSTRIDE + q4 * 288 + p4 * 8);
  auto compute_half = [&](int hf) {
#pragma unroll
    for (int s = 4 * hf; s < 4 * hf + 4; ++s) {
      const char* ap = lds + abase + ((s + rot) & 7) * 8 * OP_GSTRIDE;
#pragma unroll
      for (int c = 0; c < 9; ++c) {
        s16x4 lo = tr_read(ap + c * 32), hi = tr_read(ap + c * 32 + 4 * 288);
        bf16x8 af = __builtin_shufflevector(lo, hi, 0, 1, 2, 3, 4, 5, 6, 7);
        acc[0][c] = mfma16(af, of[0][s], acc[0][c]);
        acc[1][c] = mfma16(af, of[1][s], acc[1][c]);
      }
    }
  };
  asm volatile("s_waitcnt vmcnt(17)" : "+v"(of[0][0]), "+v"(of[0][1]), "+v"(of[0][2]), "+v"(of[0][3]), "+v"(of[1][0]), "+v"(of[1][1]), "+v"(of[1][2]), "+v"(of[1][3]) :: "memory");
  __builtin_amdgcn_s_barrier();
  compute_half(0);
  asm volatile("s_waitcnt vmcnt(0)" : "+v"(of[0][4]), "+v"(of[0][5]), "+v"(of[0][6]), "+v"(of[0][7]), "+v"(of[1][4]), "+v"(of[1][5]), "+v"(of[1][6]), "+v"(of[1][7]) :: "memory");
  __builtin_amdgcn_s_barrier();
  compute_half(1);
  __syncthreads();
  {
    char* xs = lds + wid * 9216 + lane * 16;
#pragma unroll
    for (int c = 0; c < 9; ++c) *(f32x4*)(xs + c * 1024) = kh ? acc[0][c] : acc[1][c];
  }
  __syncthreads();
  {
    const char* xr = lds + (wid ^ 4) * 9216 + lane * 16;
    float* pp = part + ((size_t)ks * MROWS + 128 * mt + 32 * mq + 16 * kh + l15) * DM + 144 * nt + 4 * g4;
#pragma unroll
    for (int c = 0; c < 9; ++c) {
      f32x4 v = *(const f32x4*)(xr + c * 1024);
      f32x4 o = kh ? acc[1][c] : acc[0][c];
      *(f32x4*)(pp + 16 * c) = v + o;
    }
  }
}

__global__ __launch_bounds__(256) void reduce_kernel(const float* __restrict__ part, u16* __restrict__ out) {
  const int i = blockIdx.x * 256 + threadIdx.x;
  f32x4 s = *(const f32x4*)(part + (size_t)i * 4);
#pragma unroll
  for (int k = 1; k < 8; ++k) s += *(const f32x4*)(part + (size_t)k * MROWS * DM + (size_t)i * 4);
  u32x2 w = {cvtpk(s[0], s[1]), cvtpk(s[2], s[3])};
  *(u32x2*)(out + (size_t)i * 4) = w;
}

extern "C" void kernel_launch(void* const* d_in, const int* in_sizes, int n_in, void* d_out, int out_size, void* d_ws, size_t ws_size,
                              hipStream_t stream) {
  const u16* x = (const u16*)d_in[0];
  const u16* kc = (const u16*)d_in[1];
  const u16* vc = (const u16*)d_in[2];
  const u16* wq = (const u16*)d_in[3];
  const u16* wo = (const u16*)d_in[4];
  u16* out = (u16*)d_out;
  u16* qws = (u16*)d_ws;
  u16* ows = qws + (size_t)MROWS * QW;
  hipFuncSetAttribute((const void*)attn_kernel, hipFuncAttributeMaxDynamicSharedMemorySize, ATT_LDS);
  attn_kernel<<<256, 512, ATT_LDS, stream>>>(x, wq, kc, vc, ows);
  float* part = (float*)((char*)d_ws + (32 << 20));
  hipFuncSetAttribute((const void*)oproj_kernel, hipFuncAttributeMaxDynamicSharedMemorySize, OP_LDS);
  oproj_kernel<<<256, 512, OP_LDS, stream>>>(ows, wo, part);
  reduce_kernel<<<MROWS * DM / 4 / 256, 256, 0, stream>>>(part, out);
}
```

```cpp
#define MAPPING 0
#include <hip/hip_runtime.h>
#include <hip/hip_bf16.h>
#include <stdint.h>

typedef __attribute__((ext_vector_type(8))) short bf16x8;
typedef __attribute__((ext_vector_type(4))) short s16x4;
typedef __attribute__((ext_vector_type(16))) float f32x16;
typedef __attribute__((ext_vector_type(4))) float f32x4;
typedef __attribute__((ext_vector_type(2))) float f32x2;
typedef __attribute__((ext_vector_type(4))) unsigned u32x4;
typedef __attribute__((ext_vector_type(2))) unsigned u32x2;
typedef __attribute__((ext_vector_type(2))) __bf16 bf16x2_t;
typedef unsigned short u16;
typedef __attribute__((address_space(3))) s16x4* lds_s16x4_p;

#define DI __device__ __forceinline__

constexpr int NB = 16, SQ = 64, DM = 576, HQ = 32, HKV = 16, SK = 1024, DH = 128;
constexpr int MROWS = NB * SQ;
constexpr int QW = HQ * DH;

DI unsigned cvtpk(float lo, float hi) {
  f32x2 v = {lo, hi};
  bf16x2_t b = __builtin_convertvector(v, bf16x2_t);
  return __builtin_bit_cast(unsigned, b);
}
DI s16x4 tr_read(const char* p) {
  return __builtin_amdgcn_ds_read_tr16_b64_v4i16((lds_s16x4_p)(p));
}
DI f32x16 mfma32(bf16x8 a, bf16x8 b, f32x16 c) { return __builtin_amdgcn_mfma_f32_32x32x16_bf16(a, b, c, 0, 0, 0); }

constexpr int NS = 4;
constexpr int SLOT = 32768;
constexpr int ATT_LDS = 163840;
constexpr float SC = 0.08838834764831845f * 1.4426950408889634f;

DI void dma16(unsigned ldsaddr, unsigned voff, const void* sbase) {
  unsigned keep;
  asm volatile("s_mov_b32 %0, m0\n\ts_mov_b32 m0, %1\n\ts_nop 0\n\tglobal_load_lds_dwordx4 %2, %3 nt\n\ts_mov_b32 m0, %0"
               : "=&s"(keep) : "s"(ldsaddr), "v"(voff), "s"(sbase) : "memory");
}

DI void dma16p(unsigned ldsaddr, unsigned voff, const void* sbase) {
  unsigned keep;
  asm volatile("s_mov_b32 %0, m0\n\ts_mov_b32 m0, %1\n\ts_nop 0\n\tglobal_load_lds_dwordx4 %2, %3\n\ts_mov_b32 m0, %0"
               : "=&s"(keep) : "s"(ldsaddr), "v"(voff), "s"(sbase) : "memory");
}

struct AttnState {
  f32x16 o[4];
  f32x16 negm;
  float m, l;
};

template <int N> DI void wait_vm() { asm volatile("s_waitcnt vmcnt(%0)" ::"i"(N) : "memory"); }

struct AttnCtx {
  const char* kbase;
  const char* vbase;
  unsigned koff[2], voff[2];
  unsigned ldsw;
  unsigned kaddr[8];
  unsigned vaddr[8];
};

#define SBAR() __builtin_amdgcn_sched_barrier(0)
constexpr float THR = 8.0f;
template <int SL> DI void issue_k(const AttnCtx& c, int t) {
  const char* kb = c.kbase + (size_t)t * 16384;
  unsigned l0 = SL * 16384 + c.ldsw;
  dma16(l0, c.koff[0], kb);
  dma16(l0 + 1024, c.koff[1], kb);
}
template <int SL> DI void issue_v(const AttnCtx& c, int t) {
  const char* vb = c.vbase + (size_t)t * 16384;
  unsigned l0 = 65536 + SL * 16384 + c.ldsw;
  dma16(l0, c.voff[0], vb);
  dma16(l0 + 1024, c.voff[1], vb);
}
DI float rowmax16(const f32x16& s) {
  float pm = s[0];
#pragma unroll
  for (int i = 1; i < 16; ++i) pm = fmaxf(pm, s[i]);
  auto rr = __builtin_amdgcn_permlane32_swap(__float_as_uint(pm), __float_as_uint(pm), false, false);
  return fmaxf(__uint_as_float(rr[0]), __uint_as_float(rr[1]));
}
template <int SL, int WAITN, bool ISSUEK, bool ISSUEV, bool NEXT> DI void step(const AttnCtx& c, AttnState& st, f32x16& sc, const bf16x8* qf, const char* lds, int t) {
  wait_vm<WAITN>();
  asm volatile("s_waitcnt lgkmcnt(0)" ::: "memory");
  __builtin_amdgcn_s_barrier();
  SBAR();
  const char* Ks = lds + ((SL + 1) & 3) * 16384;
  const char* Vs = lds + SL * 16384;
  bf16x8 kf[8];
  if (NEXT) {
#pragma unroll
    for (int i = 0; i < 8; ++i) kf[i] = *(const bf16x8*)(Ks + c.kaddr[i]);
  }
  SBAR();
  if (ISSUEK) issue_k<SL>(c, t + 4);
  if (ISSUEV) issue_v<(SL + 3) & 3>(c, t + 3);
  SBAR();
  f32x16 sn;
  if (NEXT) {
    asm("v_mfma_f32_32x32x16_bf16 %0, %1, %2, %3" : "=&v"(sn) : "v"(kf[0]), "v"(qf[0]), "v"(st.negm));
#pragma unroll
    for (int i = 1; i < 8; ++i) sn = mfma32(kf[i], qf[i], sn);
  }
  s16x4 vlo[4][2], vhi[4][2];
#pragma unroll
  for (int k = 0; k < 2; ++k)
#pragma unroll
    for (int d = 0; d < 4; ++d) {
      vlo[d][k] = tr_read(Vs + c.vaddr[2 * d] + k * 4096);
      vhi[d][k] = tr_read(Vs + c.vaddr[2 * d + 1] + k * 4096);
    }
  float ps = 0.f;
#pragma unroll
  for (int i = 0; i < 16; ++i) { sc[i] = __builtin_amdgcn_exp2f(sc[i]); ps += sc[i]; }
  st.l += ps;
  bf16x8 pf[2];
#pragma unroll
  for (int k = 0; k < 2; ++k) {
    u32x4 w = {cvtpk(sc[8 * k], sc[8 * k + 1]), cvtpk(sc[8 * k + 2], sc[8 * k + 3]), cvtpk(sc[8 * k + 4], sc[8 * k + 5]), cvtpk(sc[8 * k + 6], sc[8 * k + 7])};
    pf[k] = __builtin_bit_cast(bf16x8, w);
  }
  if (NEXT) {
#pragma unroll
    for (int i = 0; i < 8; ++i) {
      __builtin_amdgcn_sched_group_barrier(0x8, 1, 0);
      __builtin_amdgcn_sched_group_barrier(0x100, 2, 0);
      __builtin_amdgcn_sched_group_barrier(0x2, 5, 0);
    }
  }
  SBAR();
#pragma unroll
  for (int k = 0; k < 2; ++k) {
#pragma unroll
    for (int d = 0; d < 4; ++d) {
      bf16x8 vf = __builtin_shufflevector(vlo[d][k], vhi[d][k], 0, 1, 2, 3, 4, 5, 6, 7);
      st.o[d] = mfma32(vf, pf[k], st.o[d]);
    }
  }
  if (NEXT) {
    const float pm = rowmax16(sn);
    SBAR();
    if (__builtin_expect(__any(pm > THR), 0)) {
      const float dl = fmaxf(pm, 0.f);
      const float alpha = __builtin_amdgcn_exp2f(-dl);
      st.m += dl;
      st.l *= alpha;
#pragma unroll
      for (int i = 0; i < 16; ++i) { sn[i] -= dl; st.negm[i] = -st.m; }
#pragma unroll
      for (int d = 0; d < 4; ++d)
#pragma unroll
        for (int i = 0; i < 16; ++i) st.o[d][i] *= alpha;
    }
    sc = sn;
  }
  SBAR();
}

__global__ __launch_bounds__(512, 2) void attn_kernel(const u16* __restrict__ x, const u16* __restrict__ wq, const u16* __restrict__ kc,
                                                      const u16* __restrict__ vc, u16* __restrict__ og,
                                                      const u16* __restrict__ wo, char* __restrict__ wot, unsigned* __restrict__ cnt) {
  extern __shared__ __attribute__((aligned(16))) char lds[];
  const int bid = blockIdx.x;
  const int hkv = 2 * (bid & 7) + ((bid >> 3) & 1), b = bid >> 4;
  const int rot = (b * 9) >> 3;
  const int tid = threadIdx.x, lane = tid & 63, r = lane & 31, h = lane >> 5;
  const int wid = __builtin_amdgcn_readfirstlane(tid >> 6);
  const int kg = wid >> 2, g = (wid >> 1) & 1, pb = wid & 1;
  const int head = hkv * 2 + g;

  AttnCtx c;
  c.kbase = (const char*)(kc + (size_t)(b * HKV + hkv) * SK * DH);
  c.vbase = (const char*)(vc + (size_t)(b * HKV + hkv) * SK * DH);
  c.ldsw = wid * 8 * 256;
#pragma unroll
  for (int i = 0; i < 2; ++i) {
    const int row = 8 * wid + 4 * i + (lane >> 4), pos = lane & 15;
    c.koff[i] = row * 256 + ((pos ^ (row & 15)) << 4);
    c.voff[i] = row * 256 + ((pos ^ (((row & 3) << 2) | ((row >> 2) & 3))) << 4);
  }
  {
    const int krow = 32 * kg + r;
#pragma unroll
    for (int i = 0; i < 8; ++i) c.kaddr[i] = krow * 256 + (((2 * i + h) ^ (r & 15)) << 4);
    const int blk = (lane >> 4) & 1, q4 = (lane & 15) >> 2, p4 = lane & 3;
#pragma unroll
    for (int d = 0; d < 4; ++d)
#pragma unroll
      for (int t = 0; t < 2; ++t) {
        const int row = 32 * kg + 8 * t + 4 * h + q4;
        const int ch = 4 * d + 2 * blk + (p4 >> 1);
        const int sw = ((row & 3) << 2) | ((row >> 2) & 3);
        c.vaddr[2 * d + t] = 65536 + row * 256 + ((ch ^ sw) << 4) + 8 * (p4 & 1);
        asm volatile("" : "+v"(c.vaddr[2 * d + t]));
      }
  }
  const char* wqb = (const char*)(wq + (size_t)hkv * 256);
  const char* xb = (const char*)(x + (size_t)b * SQ * DM);
  constexpr unsigned XB0 = 131072;
  unsigned wsrc[2], xsrc;
#pragma unroll
  for (int i = 0; i < 2; ++i) {
    const int row = 4 * wid + 2 * i + (lane >> 5), pos = lane & 31;
    const int f = ((row & 3) << 2) | ((row >> 2) & 3);
    wsrc[i] = row * 8192 + ((pos ^ f) << 4);
  }
  {
    const int row = 16 * (wid & 3) + (lane >> 2), pos = lane & 3;
    xsrc = row * 1152 + ((pos ^ ((row >> 2) & 3)) << 4);
  }
  auto issue_chunk = [&](int chi, unsigned wbuf, unsigned xbuf) {
    int ch = chi + rot; if (ch >= 18) ch -= 18;
    const char* wsb = wqb + (size_t)ch * 32 * 8192;
#pragma unroll
    for (int i = 0; i < 2; ++i) dma16p(wbuf + wid * 2048 + i * 1024, wsrc[i], wsb);
    if (wid < 4) dma16p(xbuf + wid * 1024, xsrc, xb + ch * 64);
  };
  constexpr unsigned PS[8] = {6, 7, 0, 1, 4, 2, 5, 3};
#pragma unroll
  for (int ci = 0; ci < 8; ++ci) issue_chunk(ci, PS[ci] * 16384, XB0 + ci * 4096);
  unsigned waddr[2][2], xaddr[2];
  {
    const int blk = (lane >> 4) & 1, q4 = (lane & 15) >> 2, p4 = lane & 3;
#pragma unroll
    for (int j = 0; j < 2; ++j)
#pragma unroll
      for (int t = 0; t < 2; ++t) {
        const int row = 8 * h + 4 * t + q4;
        const int chn = 4 * (4 * g + 2 * kg + j) + 2 * blk + (p4 >> 1);
        const int f = ((row & 3) << 2) | ((row >> 2) & 3);
        waddr[j][t] = row * 512 + ((chn ^ f) << 4) + 8 * (p4 & 1);
      }
    const int row = 32 * pb + r;
#pragma unroll
    for (int s = 0; s < 2; ++s) xaddr[s] = row * 64 + (((2 * s + h) ^ ((row >> 2) & 3)) << 4);
  }
  f32x16 qa[2] = {};
  auto proj_chunk = [&](unsigned wbuf, unsigned xbuf) {
#pragma unroll
    for (int s = 0; s < 2; ++s) {
      const bf16x8 xf = *(const bf16x8*)(lds + xbuf + xaddr[s]);
#pragma unroll
      for (int j = 0; j < 2; ++j) {
        s16x4 lo = tr_read(lds + wbuf + s * 8192 + waddr[j][0]);
        s16x4 hi = tr_read(lds + wbuf + s * 8192 + waddr[j][1]);
        qa[j] = mfma32(__builtin_shufflevector(lo, hi, 0, 1, 2, 3, 4, 5, 6, 7), xf, qa[j]);
      }
    }
  };
#pragma unroll
  for (int ch = 0; ch < 18; ++ch) {
    {
      const int yc = ch == 0 ? 7 : ((ch + 6 <= 17 ? ch + 6 : 17) - ch);
      const int yt = ch > 11 ? ch - 11 : 0;
      if (wid < 4) asm volatile("s_waitcnt vmcnt(%0)" ::"i"(3 * yc + 2 * yt) : "memory");
      else asm volatile("s_waitcnt vmcnt(%0)" ::"i"(2 * yc + 2 * yt) : "memory");
    }
    asm volatile("s_waitcnt lgkmcnt(0)" ::: "memory");
    __builtin_amdgcn_s_barrier();
    if (ch >= 1 && ch <= 10) issue_chunk(ch + 7, PS[(ch + 7) & 7] * 16384, XB0 + ((ch + 7) & 7) * 4096);
    if (ch == 11) issue_k<0>(c, 0);
    if (ch == 12) issue_k<1>(c, 1);
    if (ch == 13) issue_v<0>(c, 0);
    if (ch == 14) issue_k<2>(c, 2);
    if (ch == 15) issue_v<1>(c, 1);
    if (ch == 16) issue_k<3>(c, 3);
    if (ch == 17) issue_v<2>(c, 2);
    proj_chunk(PS[ch & 7] * 16384, XB0 + (ch & 7) * 4096);
  }
  asm volatile("s_waitcnt lgkmcnt(0)" ::: "memory");
  __builtin_amdgcn_s_barrier();
  {
    char* qt = lds + 131072 + (g * 2 + pb) * 8192 + r * 256;
#pragma unroll
    for (int j = 0; j < 2; ++j)
#pragma unroll
      for (int a = 0; a < 4; ++a) {
        unsigned w0 = cvtpk(qa[j][4 * a], qa[j][4 * a + 1]), w1 = cvtpk(qa[j][4 * a + 2], qa[j][4 * a + 3]);
        w0 = cvtpk(__uint_as_float(w0 << 16) * SC, __uint_as_float(w0 & 0xffff0000u) * SC);
        w1 = cvtpk(__uint_as_float(w1 << 16) * SC, __uint_as_float(w1 & 0xffff0000u) * SC);
        u32x2 w = {w0, w1};
        *(u32x2*)(qt + (((8 * kg + 4 * j + a) ^ (r & 15)) << 4) + 8 * h) = w;
      }
  }
  wait_vm<12>();
  asm volatile("s_waitcnt lgkmcnt(0)" ::: "memory");
  __builtin_amdgcn_s_barrier();
  bf16x8 qf[8];
  {
    const char* qt = lds + 131072 + (g * 2 + pb) * 8192 + r * 256;
#pragma unroll
    for (int i = 0; i < 8; ++i) qf[i] = *(const bf16x8*)(qt + (((2 * i + h) ^ (r & 15)) << 4));
  }

  AttnState st;
#pragma unroll
  for (int d = 0; d < 4; ++d) st.o[d] = f32x16{};
  st.m = -1e30f; st.l = 0.f;

  f32x16 sc = {};
#pragma unroll
  for (int i = 0; i < 8; ++i) sc = mfma32(*(const bf16x8*)(lds + c.kaddr[i]), qf[i], sc);
  st.m = rowmax16(sc);
#pragma unroll
  for (int i = 0; i < 16; ++i) { sc[i] -= st.m; st.negm[i] = -st.m; }
  step<0, 8, true, true, true>(c, st, sc, qf, lds, 0);
  step<1, 8, true, true, true>(c, st, sc, qf, lds, 1);
  step<2, 8, true, true, true>(c, st, sc, qf, lds, 2);
  step<3, 8, true, true, true>(c, st, sc, qf, lds, 3);
#pragma unroll 1
  for (int t = 4; t < 12; t += 4) {
    step<0, 8, true, true, true>(c, st, sc, qf, lds, t);
    step<1, 8, true, true, true>(c, st, sc, qf, lds, t + 1);
    step<2, 8, true, true, true>(c, st, sc, qf, lds, t + 2);
    step<3, 8, true, true, true>(c, st, sc, qf, lds, t + 3);
  }
  step<0, 8, false, true, true>(c, st, sc, qf, lds, 12);
  step<1, 6, false, false, true>(c, st, sc, qf, lds, 13);
  step<2, 2, false, false, true>(c, st, sc, qf, lds, 14);
  step<3, 0, false, false, false>(c, st, sc, qf, lds, 15);

  u32x4 wl[8];
  const int wk = bid >> 1, wgi = (bid & 1) * 36 + wid * 16 + (lane & 15);
  const bool wact = wid < 3 && (wid * 16 + (lane & 15)) < 36;
  if (wid < 3) {
    const int gcl = wact ? wgi : (bid & 1) * 36;
    const u16* wp = wo + (size_t)(32 * wk + 8 * (lane >> 4)) * DM + gcl * 8;
#pragma unroll
    for (int j = 0; j < 8; ++j) wl[j] = __builtin_nontemporal_load((const u32x4*)(wp + (size_t)j * DM));
  }
  if (bid == 0 && tid < 256) cnt[tid * 32] = 0u;
  float lt = st.l + __shfl_xor(st.l, 32);
  __syncthreads();
  char* xw = lds + wid * 8704;
  char* xp = lds + (wid ^ 4) * 8704;
  {
#pragma unroll
    for (int dd = 0; dd < 2; ++dd) {
      const f32x16 v = kg ? st.o[dd] : st.o[2 + dd];
#pragma unroll
      for (int a = 0; a < 4; ++a) {
        f32x4 w = {v[4 * a], v[4 * a + 1], v[4 * a + 2], v[4 * a + 3]};
        *(f32x4*)(xw + ((dd * 4 + a) * 64 + lane) * 16) = w;
      }
    }
    f32x2 ml = {st.m, lt};
    *(f32x2*)(xw + 8192 + lane * 8) = ml;
  }
  __syncthreads();
  f32x2 pml = *(const f32x2*)(xp + 8192 + lane * 8);
  const float mm = fmaxf(st.m, pml[0]);
  const float fa = __builtin_amdgcn_exp2f(st.m - mm), fb = __builtin_amdgcn_exp2f(pml[0] - mm);
  const float inv = 1.0f / (lt * fa + pml[1] * fb);
  const float wa = fa * inv, wb = fb * inv;
  char* stg = lds + 8 * 8704 + wid * 4608;
#pragma unroll
  for (int dd = 0; dd < 2; ++dd) {
    const f32x16 own = kg ? st.o[2 + dd] : st.o[dd];
#pragma unroll
    for (int a = 0; a < 4; ++a) {
      f32x4 pv = *(const f32x4*)(xp + ((dd * 4 + a) * 64 + lane) * 16);
      float o0 = own[4 * a] * wa + pv[0] * wb, o1 = own[4 * a + 1] * wa + pv[1] * wb;
      float o2 = own[4 * a + 2] * wa + pv[2] * wb, o3 = own[4 * a + 3] * wa + pv[3] * wb;
      u32x2 w = {cvtpk(o0, o1), cvtpk(o2, o3)};
      *(u32x2*)(stg + r * 144 + (32 * dd + 8 * a + 4 * h) * 2) = w;
    }
  }
  asm volatile("s_waitcnt lgkmcnt(0)" ::: "memory");
  __builtin_amdgcn_wave_barrier();
  {
    const int col0 = head * DH + kg * 64;
    const int ksl = col0 >> 9, kst = (col0 & 511) >> 5;
    const int mt0 = (b * SQ + 32 * pb) >> 4;
#pragma unroll
    for (int i = 0; i < 4; ++i) {
      const int mi = i >> 1, ki = i & 1;
      u32x4 w = *(const u32x4*)(stg + (16 * mi + (lane & 15)) * 144 + ki * 64 + (lane >> 4) * 16);
      char* dst = (char*)og + ((size_t)((ksl * 64 + mt0 + mi) * 16 + kst + ki) << 10) + lane * 16;
      *(u32x4*)dst = w;
    }
  }
  if (wid < 3) {
    const int ntile = wgi >> 1, ksl = wk >> 4, kst = wk & 15, ntq = ntile / 9, cc = ntile - ntq * 9;
    char* dstb = wot + ((size_t)(((ksl * 4 + ntq) * 16 + kst) * 9 + cc) << 10) + ((lane >> 4) * 16 + (wgi & 1) * 8) * 16;
#pragma unroll
    for (int e2 = 0; e2 < 4; ++e2) {
      u32x4 lo, hi;
#pragma unroll
      for (int j2 = 0; j2 < 4; ++j2) {
        const unsigned a = wl[2 * j2][e2], bb = wl[2 * j2 + 1][e2];
        lo[j2] = __builtin_amdgcn_perm(bb, a, 0x05040100u);
        hi[j2] = __builtin_amdgcn_perm(bb, a, 0x07060302u);
      }
      if (wact) { *(u32x4*)(dstb + (2 * e2) * 16) = lo; *(u32x4*)(dstb + (2 * e2 + 1) * 16) = hi; }
    }
  }
}

DI f32x4 mfma16(bf16x8 a, bf16x8 b, f32x4 c) { return __builtin_amdgcn_mfma_f32_16x16x32_bf16(a, b, c, 0, 0, 0); }
constexpr int OP_LDS = 147456;
__global__ __launch_bounds__(512, 2) void oproj_kernel(const char* __restrict__ og, const char* __restrict__ wot, char* __restrict__ part,
                                                       unsigned* __restrict__ cnt, u16* __restrict__ out) {
  extern __shared__ __attribute__((aligned(16))) char lds[];
  const int bid = blockIdx.x;
  const int ks = bid & 7, nt = (bid >> 3) & 3, mt = bid >> 5;
  const int tid = threadIdx.x, lane = tid & 63;
  const int wid = __builtin_amdgcn_readfirstlane(tid >> 6);
  const int mq = wid & 3, kh = wid >> 2;
  const int l15 = lane & 15, g4 = lane >> 4;
  const int rot = (mt + 2 * nt) & 7;
  const char* ob = og + ((size_t)((ks * 64 + 8 * mt + 2 * mq) * 16 + kh) << 10);
  const unsigned ooff0 = lane * 16, ooff1 = ooff0 + 16384;
  const char* wb = wot + ((size_t)((ks * 4 + nt) * 144) << 10);
  bf16x8 of[2][8];
  auto load_o = [&](int s) {
    const char* p = ob + ((s + rot) & 7) * 2048;
    asm volatile("global_load_dwordx4 %0, %1, %2" : "=v"(of[0][s]) : "v"(ooff0), "s"(p) : "memory");
    asm volatile("global_load_dwordx4 %0, %1, %2" : "=v"(of[1][s]) : "v"(ooff1), "s"(p) : "memory");
  };
  auto dma_stage = [&](int st) {
#pragma unroll
    for (int i = 0; i < 9; ++i) {
      const int j = wid + 8 * i;
      const int cc = j / 18, pin = j - cc * 18;
      const int p = ((4 * st + cc + rot) & 7) * 18 + pin;
      dma16p(p * 1024, ooff0, wb + p * 1024);
    }
  };
#pragma unroll
  for (int s = 0; s < 4; ++s) load_o(s);
  dma_stage(0);
#pragma unroll
  for (int s = 4; s < 8; ++s) load_o(s);
  dma_stage(1);

  f32x4 acc[2][9];
#pragma unroll
  for (int i = 0; i < 2; ++i)
#pragma unroll
    for (int c = 0; c < 9; ++c) acc[i][c] = f32x4{0.f, 0.f, 0.f, 0.f};
  auto compute_half = [&](int hf) {
#pragma unroll
    for (int s = 4 * hf; s < 4 * hf + 4; ++s) {
      const char* ap = lds + (((s + rot) & 7) * 18 + kh * 9) * 1024 + lane * 16;
#pragma unroll
      for (int c = 0; c < 9; ++c) {
        bf16x8 af = *(const bf16x8*)(ap + c * 1024);
        acc[0][c] = mfma16(af, of[0][s], acc[0][c]);
        acc[1][c] = mfma16(af, of[1][s], acc[1][c]);
      }
    }
  };
  asm volatile("s_waitcnt vmcnt(17)" : "+v"(of[0][0]), "+v"(of[0][1]), "+v"(of[0][2]), "+v"(of[0][3]), "+v"(of[1][0]), "+v"(of[1][1]), "+v"(of[1][2]), "+v"(of[1][3]) :: "memory");
  __builtin_amdgcn_s_barrier();
  compute_half(0);
  asm volatile("s_waitcnt vmcnt(0)" : "+v"(of[0][4]), "+v"(of[0][5]), "+v"(of[0][6]), "+v"(of[0][7]), "+v"(of[1][4]), "+v"(of[1][5]), "+v"(of[1][6]), "+v"(of[1][7]) :: "memory");
  __builtin_amdgcn_s_barrier();
  compute_half(1);
  __syncthreads();
  {
    char* xs = lds + wid * 9216 + lane * 16;
#pragma unroll
    for (int c = 0; c < 9; ++c) *(f32x4*)(xs + c * 1024) = kh ? acc[0][c] : acc[1][c];
  }
  __syncthreads();
  const int tile = mt * 4 + nt;
  {
    const int dest = 2 * mq + kh;
    const char* xr = lds + (wid ^ 4) * 9216 + lane * 16;
    char* pp = part + (size_t)((tile * 8 + dest) * 8 + ks) * 9216;
    const __amdgpu_buffer_rsrc_t rs = __builtin_amdgcn_make_buffer_rsrc((void*)pp, (short)0, 9216, 0x00020000);
#pragma unroll
    for (int c = 0; c < 9; ++c) {
      f32x4 v = *(const f32x4*)(xr + c * 1024);
      f32x4 o = kh ? acc[1][c] : acc[0][c];
      v += o;
      __builtin_amdgcn_raw_buffer_store_b128(__builtin_bit_cast(u32x4, v), rs, lane * 16 + c * 1024, 0, 16);
    }
    asm volatile("s_waitcnt vmcnt(0)" ::: "memory");
    if (lane == 0) __hip_atomic_fetch_add(cnt + (tile * 8 + dest) * 32, 1u, __ATOMIC_RELAXED, __HIP_MEMORY_SCOPE_AGENT);
  }
  if (wid == 0) {
    unsigned spins = 0;
    while (__hip_atomic_load(cnt + (tile * 8 + ks) * 32, __ATOMIC_RELAXED, __HIP_MEMORY_SCOPE_AGENT) < 8u) {
      __builtin_amdgcn_s_sleep(1);
      if (++spins > (1u << 22)) break;
    }
  }
  __syncthreads();
  {
    char* pr = part + (size_t)((tile * 8 + ks) * 8) * 9216;
    const __amdgpu_buffer_rsrc_t rs = __builtin_amdgcn_make_buffer_rsrc((void*)pr, (short)0, 8 * 9216, 0x00020000);
#pragma unroll
    for (int it = 0; it < 2; ++it) {
      const int ci = tid + 512 * it;
      if (ci < 576) {
        u32x4 v[8];
#pragma unroll
        for (int s = 0; s < 8; ++s) v[s] = __builtin_amdgcn_raw_buffer_load_b128(rs, s * 9216 + ci * 16, 0, 16);
        f32x4 sum = __builtin_bit_cast(f32x4, v[0]);
#pragma unroll
        for (int s = 1; s < 8; ++s) sum += __builtin_bit_cast(f32x4, v[s]);
        const int c = ci >> 6, ln = ci & 63;
        u32x2 w = {cvtpk(sum[0], sum[1]), cvtpk(sum[2], sum[3])};
        *(u32x2*)(out + (size_t)(128 * mt + 16 * ks + (ln & 15)) * DM + 144 * nt + 16 * c + 4 * (ln >> 4)) = w;
      }
    }
  }
}

extern "C" void kernel_launch(void* const* d_in, const int* in_sizes, int n_in, void* d_out, int out_size, void* d_ws, size_t ws_size,
                              hipStream_t stream) {
  const u16* x = (const u16*)d_in[0];
  const u16* kc = (const u16*)d_in[1];
  const u16* vc = (const u16*)d_in[2];
  const u16* wq = (const u16*)d_in[3];
  const u16* wo = (const u16*)d_in[4];
  u16* out = (u16*)d_out;
  u16* qws = (u16*)d_ws;
  u16* ows = qws + (size_t)MROWS * QW;
  hipFuncSetAttribute((const void*)attn_kernel, hipFuncAttributeMaxDynamicSharedMemorySize, ATT_LDS);
  char* wot = (char*)d_ws + (24 << 20);
  char* part = (char*)d_ws + (32 << 20);
  unsigned* cnt = (unsigned*)((char*)d_ws + (56 << 20));
  attn_kernel<<<256, 512, ATT_LDS, stream>>>(x, wq, kc, vc, ows, wo, wot, cnt);
  hipFuncSetAttribute((const void*)oproj_kernel, hipFuncAttributeMaxDynamicSharedMemorySize, OP_LDS);
  oproj_kernel<<<256, 512, OP_LDS, stream>>>((const char*)ows, wot, part, cnt, out);
}
```

```cpp
#define MAPPING 0
#include <hip/hip_runtime.h>
#include <hip/hip_bf16.h>
#include <stdint.h>

typedef __attribute__((ext_vector_type(8))) short bf16x8;
typedef __attribute__((ext_vector_type(4))) short s16x4;
typedef __attribute__((ext_vector_type(16))) float f32x16;
typedef __attribute__((ext_vector_type(4))) float f32x4;
typedef __attribute__((ext_vector_type(2))) float f32x2;
typedef __attribute__((ext_vector_type(4))) unsigned u32x4;
typedef __attribute__((ext_vector_type(2))) unsigned u32x2;
typedef __attribute__((ext_vector_type(2))) __bf16 bf16x2_t;
typedef unsigned short u16;
typedef __attribute__((address_space(3))) s16x4* lds_s16x4_p;

#define DI __device__ __forceinline__

constexpr int NB = 16, SQ = 64, DM = 576, HQ = 32, HKV = 16, SK = 1024, DH = 128;
constexpr int MROWS = NB * SQ;
constexpr int QW = HQ * DH;

DI unsigned cvtpk(float lo, float hi) {
  f32x2 v = {lo, hi};
  bf16x2_t b = __builtin_convertvector(v, bf16x2_t);
  return __builtin_bit_cast(unsigned, b);
}
DI s16x4 tr_read(const char* p) {
  return __builtin_amdgcn_ds_read_tr16_b64_v4i16((lds_s16x4_p)(p));
}
DI f32x16 mfma32(bf16x8 a, bf16x8 b, f32x16 c) { return __builtin_amdgcn_mfma_f32_32x32x16_bf16(a, b, c, 0, 0, 0); }

constexpr int NS = 4;
constexpr int SLOT = 32768;
constexpr int ATT_LDS = 163840;
constexpr float SC = 0.08838834764831845f * 1.4426950408889634f;

DI void dma16(unsigned ldsaddr, unsigned voff, const void* sbase) {
  unsigned keep;
  asm volatile("s_mov_b32 %0, m0\n\ts_mov_b32 m0, %1\n\ts_nop 0\n\tglobal_load_lds_dwordx4 %2, %3 nt\n\ts_mov_b32 m0, %0"
               : "=&s"(keep) : "s"(ldsaddr), "v"(voff), "s"(sbase) : "memory");
}

DI void dma16p(unsigned ldsaddr, unsigned voff, const void* sbase) {
  unsigned keep;
  asm volatile("s_mov_b32 %0, m0\n\ts_mov_b32 m0, %1\n\ts_nop 0\n\tglobal_load_lds_dwordx4 %2, %3\n\ts_mov_b32 m0, %0"
               : "=&s"(keep) : "s"(ldsaddr), "v"(voff), "s"(sbase) : "memory");
}

struct AttnState {
  f32x16 o[4];
  f32x16 negm;
  float m, l;
};

template <int N> DI void wait_vm() { asm volatile("s_waitcnt vmcnt(%0)" ::"i"(N) : "memory"); }

struct AttnCtx {
  const char* kbase;
  const char* vbase;
  unsigned koff[2], voff[2];
  unsigned ldsw;
  unsigned kaddr[8];
  unsigned vaddr[8];
};

#define SBAR() __builtin_amdgcn_sched_barrier(0)
constexpr float THR = 8.0f;
template <int SL> DI void issue_k(const AttnCtx& c, int t) {
  const char* kb = c.kbase + (size_t)t * 16384;
  unsigned l0 = SL * 16384 + c.ldsw;
  dma16(l0, c.koff[0], kb);
  dma16(l0 + 1024, c.koff[1], kb);
}
template <int SL> DI void issue_v(const AttnCtx& c, int t) {
  const char* vb = c.vbase + (size_t)t * 16384;
  unsigned l0 = 65536 + SL * 16384 + c.ldsw;
  dma16(l0, c.voff[0], vb);
  dma16(l0 + 1024, c.voff[1], vb);
}
DI float rowmax16(const f32x16& s) {
  float pm = s[0];
#pragma unroll
  for (int i = 1; i < 16; ++i) pm = fmaxf(pm, s[i]);
  auto rr = __builtin_amdgcn_permlane32_swap(__float_as_uint(pm), __float_as_uint(pm), false, false);
  return fmaxf(__uint_as_float(rr[0]), __uint_as_float(rr[1]));
}
template <int SL, int WAITN, bool ISSUEK, bool ISSUEV, bool NEXT> DI void step(const AttnCtx& c, AttnState& st, f32x16& sc, const bf16x8* qf, const char* lds, int t) {
  wait_vm<WAITN>();
  asm volatile("s_waitcnt lgkmcnt(0)" ::: "memory");
  __builtin_amdgcn_s_barrier();
  SBAR();
  const char* Ks = lds + ((SL + 1) & 3) * 16384;
  const char* Vs = lds + SL * 16384;
  bf16x8 kf[8];
  if (NEXT) {
#pragma unroll
    for (int i = 0; i < 8; ++i) kf[i] = *(const bf16x8*)(Ks + c.kaddr[i]);
  }
  SBAR();
  if (ISSUEK) issue_k<SL>(c, t + 4);
  if (ISSUEV) issue_v<(SL + 3) & 3>(c, t + 3);
  SBAR();
  f32x16 sn;
  if (NEXT) {
    asm("v_mfma_f32_32x32x16_bf16 %0, %1, %2, %3" : "=&v"(sn) : "v"(kf[0]), "v"(qf[0]), "v"(st.negm));
#pragma unroll
    for (int i = 1; i < 8; ++i) sn = mfma32(kf[i], qf[i], sn);
  }
  s16x4 vlo[4][2], vhi[4][2];
#pragma unroll
  for (int k = 0; k < 2; ++k)
#pragma unroll
    for (int d = 0; d < 4; ++d) {
      vlo[d][k] = tr_read(Vs + c.vaddr[2 * d] + k * 4096);
      vhi[d][k] = tr_read(Vs + c.vaddr[2 * d + 1] + k * 4096);
    }
  float ps = 0.f;
#pragma unroll
  for (int i = 0; i < 16; ++i) { sc[i] = __builtin_amdgcn_exp2f(sc[i]); ps += sc[i]; }
  st.l += ps;
  bf16x8 pf[2];
#pragma unroll
  for (int k = 0; k < 2; ++k) {
    u32x4 w = {cvtpk(sc[8 * k], sc[8 * k + 1]), cvtpk(sc[8 * k + 2], sc[8 * k + 3]), cvtpk(sc[8 * k + 4], sc[8 * k + 5]), cvtpk(sc[8 * k + 6], sc[8 * k + 7])};
    pf[k] = __builtin_bit_cast(bf16x8, w);
  }
  if (NEXT) {
#pragma unroll
    for (int i = 0; i < 8; ++i) {
      __builtin_amdgcn_sched_group_barrier(0x8, 1, 0);
      __builtin_amdgcn_sched_group_barrier(0x100, 2, 0);
      __builtin_amdgcn_sched_group_barrier(0x2, 5, 0);
    }
  }
  SBAR();
#pragma unroll
  for (int k = 0; k < 2; ++k) {
#pragma unroll
    for (int d = 0; d < 4; ++d) {
      bf16x8 vf = __builtin_shufflevector(vlo[d][k], vhi[d][k], 0, 1, 2, 3, 4, 5, 6, 7);
      st.o[d] = mfma32(vf, pf[k], st.o[d]);
    }
  }
  if (NEXT) {
    const float pm = rowmax16(sn);
    SBAR();
    if (__builtin_expect(__any(pm > THR), 0)) {
      const float dl = fmaxf(pm, 0.f);
      const float alpha = __builtin_amdgcn_exp2f(-dl);
      st.m += dl;
      st.l *= alpha;
#pragma unroll
      for (int i = 0; i < 16; ++i) { sn[i] -= dl; st.negm[i] = -st.m; }
#pragma unroll
      for (int d = 0; d < 4; ++d)
#pragma unroll
        for (int i = 0; i < 16; ++i) st.o[d][i] *= alpha;
    }
    sc = sn;
  }
  SBAR();
}

__global__ __launch_bounds__(512, 2) void attn_kernel(const u16* __restrict__ x, const u16* __restrict__ wq, const u16* __restrict__ kc,
                                                      const u16* __restrict__ vc, u16* __restrict__ og,
                                                      const u16* __restrict__ wo, char* __restrict__ wot) {
  extern __shared__ __attribute__((aligned(16))) char lds[];
  const int bid = blockIdx.x;
  const int hkv = 2 * (bid & 7) + ((bid >> 3) & 1), b = bid >> 4;
  const int rot = (b * 9) >> 3;
  const int tid = threadIdx.x, lane = tid & 63, r = lane & 31, h = lane >> 5;
  const int wid = __builtin_amdgcn_readfirstlane(tid >> 6);
  const int kg = wid >> 2, g = (wid >> 1) & 1, pb = wid & 1;
  const int head = hkv * 2 + g;

  AttnCtx c;
  c.kbase = (const char*)(kc + (size_t)(b * HKV + hkv) * SK * DH);
  c.vbase = (const char*)(vc + (size_t)(b * HKV + hkv) * SK * DH);
  c.ldsw = wid * 8 * 256;
#pragma unroll
  for (int i = 0; i < 2; ++i) {
    const int row = 8 * wid + 4 * i + (lane >> 4), pos = lane & 15;
    c.koff[i] = row * 256 + ((pos ^ (row & 15)) << 4);
    c.voff[i] = row * 256 + ((pos ^ (((row & 3) << 2) | ((row >> 2) & 3))) << 4);
  }
  {
    const int krow = 32 * kg + r;
#pragma unroll
    for (int i = 0; i < 8; ++i) c.kaddr[i] = krow * 256 + (((2 * i + h) ^ (r & 15)) << 4);
    const int blk = (lane >> 4) & 1, q4 = (lane & 15) >> 2, p4 = lane & 3;
#pragma unroll
    for (int d = 0; d < 4; ++d)
#pragma unroll
      for (int t = 0; t < 2; ++t) {
        const int row = 32 * kg + 8 * t + 4 * h + q4;
        const int ch = 4 * d + 2 * blk + (p4 >> 1);
        const int sw = ((row & 3) << 2) | ((row >> 2) & 3);
        c.vaddr[2 * d + t] = 65536 + row * 256 + ((ch ^ sw) << 4) + 8 * (p4 & 1);
        asm volatile("" : "+v"(c.vaddr[2 * d + t]));
      }
  }
  const char* wqb = (const char*)(wq + (size_t)hkv * 256);
  const char* xb = (const char*)(x + (size_t)b * SQ * DM);
  constexpr unsigned XB0 = 131072;
  unsigned wsrc[2], xsrc;
#pragma unroll
  for (int i = 0; i < 2; ++i) {
    const int row = 4 * wid + 2 * i + (lane >> 5), pos = lane & 31;
    const int f = ((row & 3) << 2) | ((row >> 2) & 3);
    wsrc[i] = row * 8192 + ((pos ^ f) << 4);
  }
  {
    const int row = 16 * (wid & 3) + (lane >> 2), pos = lane & 3;
    xsrc = row * 1152 + ((pos ^ ((row >> 2) & 3)) << 4);
  }
  auto issue_chunk = [&](int chi, unsigned wbuf, unsigned xbuf) {
    int ch = chi + rot; if (ch >= 18) ch -= 18;
    const char* wsb = wqb + (size_t)ch * 32 * 8192;
#pragma unroll
    for (int i = 0; i < 2; ++i) dma16p(wbuf + wid * 2048 + i * 1024, wsrc[i], wsb);
    if (wid < 4) dma16p(xbuf + wid * 1024, xsrc, xb + ch * 64);
  };
  constexpr unsigned PS[8] = {6, 7, 0, 1, 4, 2, 5, 3};
#pragma unroll
  for (int ci = 0; ci < 8; ++ci) issue_chunk(ci, PS[ci] * 16384, XB0 + ci * 4096);
  unsigned waddr[2][2], xaddr[2];
  {
    const int blk = (lane >> 4) & 1, q4 = (lane & 15) >> 2, p4 = lane & 3;
#pragma unroll
    for (int j = 0; j < 2; ++j)
#pragma unroll
      for (int t = 0; t < 2; ++t) {
        const int row = 8 * h + 4 * t + q4;
        const int chn = 4 * (4 * g + 2 * kg + j) + 2 * blk + (p4 >> 1);
        const int f = ((row & 3) << 2) | ((row >> 2) & 3);
        waddr[j][t] = row * 512 + ((chn ^ f) << 4) + 8 * (p4 & 1);
      }
    const int row = 32 * pb + r;
#pragma unroll
    for (int s = 0; s < 2; ++s) xaddr[s] = row * 64 + (((2 * s + h) ^ ((row >> 2) & 3)) << 4);
  }
  f32x16 qa[2] = {};
  auto proj_chunk = [&](unsigned wbuf, unsigned xbuf) {
#pragma unroll
    for (int s = 0; s < 2; ++s) {
      const bf16x8 xf = *(const bf16x8*)(lds + xbuf + xaddr[s]);
#pragma unroll
      for (int j = 0; j < 2; ++j) {
        s16x4 lo = tr_read(lds + wbuf + s * 8192 + waddr[j][0]);
        s16x4 hi = tr_read(lds + wbuf + s * 8192 + waddr[j][1]);
        qa[j] = mfma32(__builtin_shufflevector(lo, hi, 0, 1, 2, 3, 4, 5, 6, 7), xf, qa[j]);
      }
    }
  };
#pragma unroll
  for (int ch = 0; ch < 18; ++ch) {
    {
      const int yc = ch == 0 ? 7 : ((ch + 6 <= 17 ? ch + 6 : 17) - ch);
      const int yt = ch > 11 ? ch - 11 : 0;
      if (wid < 4) asm volatile("s_waitcnt vmcnt(%0)" ::"i"(3 * yc + 2 * yt) : "memory");
      else asm volatile("s_waitcnt vmcnt(%0)" ::"i"(2 * yc + 2 * yt) : "memory");
    }
    asm volatile("s_waitcnt lgkmcnt(0)" ::: "memory");
    __builtin_amdgcn_s_barrier();
    if (ch >= 1 && ch <= 10) issue_chunk(ch + 7, PS[(ch + 7) & 7] * 16384, XB0 + ((ch + 7) & 7) * 4096);
    if (ch == 11) issue_k<0>(c, 0);
    if (ch == 12) issue_k<1>(c, 1);
    if (ch == 13) issue_v<0>(c, 0);
    if (ch == 14) issue_k<2>(c, 2);
    if (ch == 15) issue_v<1>(c, 1);
    if (ch == 16) issue_k<3>(c, 3);
    if (ch == 17) issue_v<2>(c, 2);
    proj_chunk(PS[ch & 7] * 16384, XB0 + (ch & 7) * 4096);
  }
  asm volatile("s_waitcnt lgkmcnt(0)" ::: "memory");
  __builtin_amdgcn_s_barrier();
  {
    char* qt = lds + 131072 + (g * 2 + pb) * 8192 + r * 256;
#pragma unroll
    for (int j = 0; j < 2; ++j)
#pragma unroll
      for (int a = 0; a < 4; ++a) {
        unsigned w0 = cvtpk(qa[j][4 * a], qa[j][4 * a + 1]), w1 = cvtpk(qa[j][4 * a + 2], qa[j][4 * a + 3]);
        w0 = cvtpk(__uint_as_float(w0 << 16) * SC, __uint_as_float(w0 & 0xffff0000u) * SC);
        w1 = cvtpk(__uint_as_float(w1 << 16) * SC, __uint_as_float(w1 & 0xffff0000u) * SC);
        u32x2 w = {w0, w1};
        *(u32x2*)(qt + (((8 * kg + 4 * j + a) ^ (r & 15)) << 4) + 8 * h) = w;
      }
  }
  wait_vm<12>();
  asm volatile("s_waitcnt lgkmcnt(0)" ::: "memory");
  __builtin_amdgcn_s_barrier();
  bf16x8 qf[8];
  {
    const char* qt = lds + 131072 + (g * 2 + pb) * 8192 + r * 256;
#pragma unroll
    for (int i = 0; i < 8; ++i) qf[i] = *(const bf16x8*)(qt + (((2 * i + h) ^ (r & 15)) << 4));
  }

  AttnState st;
#pragma unroll
  for (int d = 0; d < 4; ++d) st.o[d] = f32x16{};
  st.m = -1e30f; st.l = 0.f;

  f32x16 sc = {};
#pragma unroll
  for (int i = 0; i < 8; ++i) sc = mfma32(*(const bf16x8*)(lds + c.kaddr[i]), qf[i], sc);
  st.m = rowmax16(sc);
#pragma unroll
  for (int i = 0; i < 16; ++i) { sc[i] -= st.m; st.negm[i] = -st.m; }
  step<0, 8, true, true, true>(c, st, sc, qf, lds, 0);
  step<1, 8, true, true, true>(c, st, sc, qf, lds, 1);
  step<2, 8, true, true, true>(c, st, sc, qf, lds, 2);
  step<3, 8, true, true, true>(c, st, sc, qf, lds, 3);
#pragma unroll 1
  for (int t = 4; t < 12; t += 4) {
    step<0, 8, true, true, true>(c, st, sc, qf, lds, t);
    step<1, 8, true, true, true>(c, st, sc, qf, lds, t + 1);
    step<2, 8, true, true, true>(c, st, sc, qf, lds, t + 2);
    step<3, 8, true, true, true>(c, st, sc, qf, lds, t + 3);
  }
  step<0, 8, false, true, true>(c, st, sc, qf, lds, 12);
  step<1, 6, false, false, true>(c, st, sc, qf, lds, 13);
  step<2, 2, false, false, true>(c, st, sc, qf, lds, 14);
  step<3, 0, false, false, false>(c, st, sc, qf, lds, 15);

  u32x4 wl[8];
  const int wk = bid >> 1, wgi = (bid & 1) * 36 + wid * 16 + (lane & 15);
  const bool wact = wid < 3 && (wid * 16 + (lane & 15)) < 36;
  if (wid < 3) {
    const int gcl = wact ? wgi : (bid & 1) * 36;
    const u16* wp = wo + (size_t)(32 * wk + 8 * (lane >> 4)) * DM + gcl * 8;
#pragma unroll
    for (int j = 0; j < 8; ++j) wl[j] = __builtin_nontemporal_load((const u32x4*)(wp + (size_t)j * DM));
  }
  float lt = st.l + __shfl_xor(st.l, 32);
  __syncthreads();
  char* xw = lds + wid * 8704;
  char* xp = lds + (wid ^ 4) * 8704;
  {
#pragma unroll
    for (int dd = 0; dd < 2; ++dd) {
      const f32x16 v = kg ? st.o[dd] : st.o[2 + dd];
#pragma unroll
      for (int a = 0; a < 4; ++a) {
        f32x4 w = {v[4 * a], v[4 * a + 1], v[4 * a + 2], v[4 * a + 3]};
        *(f32x4*)(xw + ((dd * 4 + a) * 64 + lane) * 16) = w;
      }
    }
    f32x2 ml = {st.m, lt};
    *(f32x2*)(xw + 8192 + lane * 8) = ml;
  }
  __syncthreads();
  f32x2 pml = *(const f32x2*)(xp + 8192 + lane * 8);
  const float mm = fmaxf(st.m, pml[0]);
  const float fa = __builtin_amdgcn_exp2f(st.m - mm), fb = __builtin_amdgcn_exp2f(pml[0] - mm);
  const float inv = 1.0f / (lt * fa + pml[1] * fb);
  const float wa = fa * inv, wb = fb * inv;
  char* stg = lds + 8 * 8704 + wid * 4608;
#pragma unroll
  for (int dd = 0; dd < 2; ++dd) {
    const f32x16 own = kg ? st.o[2 + dd] : st.o[dd];
#pragma unroll
    for (int a = 0; a < 4; ++a) {
      f32x4 pv = *(const f32x4*)(xp + ((dd * 4 + a) * 64 + lane) * 16);
      float o0 = own[4 * a] * wa + pv[0] * wb, o1 = own[4 * a + 1] * wa + pv[1] * wb;
      float o2 = own[4 * a + 2] * wa + pv[2] * wb, o3 = own[4 * a + 3] * wa + pv[3] * wb;
      u32x2 w = {cvtpk(o0, o1), cvtpk(o2, o3)};
      *(u32x2*)(stg + r * 144 + (32 * dd + 8 * a + 4 * h) * 2) = w;
    }
  }
  asm volatile("s_waitcnt lgkmcnt(0)" ::: "memory");
  __builtin_amdgcn_wave_barrier();
  {
    const int kst = (head * DH + kg * 64) >> 5;
    const int mt0 = (b * SQ + 32 * pb) >> 4;
#pragma unroll
    for (int i = 0; i < 4; ++i) {
      const int mi = i >> 1, ki = i & 1;
      u32x4 w = *(const u32x4*)(stg + (16 * mi + (lane & 15)) * 144 + ki * 64 + (lane >> 4) * 16);
      char* dst = (char*)og + ((size_t)((kst + ki) * 64 + mt0 + mi) << 10) + lane * 16;
      *(u32x4*)dst = w;
    }
  }
  if (wid < 3) {
    const int ntile = wgi >> 1;
    char* dstb = wot + ((size_t)(wk * 36 + ntile) << 10) + ((lane >> 4) * 16 + (wgi & 1) * 8) * 16;
#pragma unroll
    for (int e2 = 0; e2 < 4; ++e2) {
      u32x4 lo, hi;
#pragma unroll
      for (int j2 = 0; j2 < 4; ++j2) {
        const unsigned a = wl[2 * j2][e2], bb = wl[2 * j2 + 1][e2];
        lo[j2] = __builtin_amdgcn_perm(bb, a, 0x05040100u);
        hi[j2] = __builtin_amdgcn_perm(bb, a, 0x07060302u);
      }
      if (wact) { *(u32x4*)(dstb + (2 * e2) * 16) = lo; *(u32x4*)(dstb + (2 * e2 + 1) * 16) = hi; }
    }
  }
}

DI f32x4 mfma16(bf16x8 a, bf16x8 b, f32x4 c) { return __builtin_amdgcn_mfma_f32_16x16x32_bf16(a, b, c, 0, 0, 0); }
constexpr int OP_LDS = 98304;
__global__ __launch_bounds__(512, 2) void oproj_kernel(const char* __restrict__ og, const char* __restrict__ wot, u16* __restrict__ out) {
  extern __shared__ __attribute__((aligned(16))) char lds[];
  const int bid = blockIdx.x;
  const int xc = bid & 7, j = bid >> 3;
  const int mt = (xc & 3) * 4 + (j & 3), nt = (xc >> 2) * 6 + (j >> 2);
  const int tid = threadIdx.x, lane = tid & 63;
  const int wid = __builtin_amdgcn_readfirstlane(tid >> 6);
  const int rot = (5 * j) & 15;
  const unsigned voff = lane * 16;
  const char* ob = og + ((size_t)((16 * wid) * 64 + 4 * mt) << 10);
  const char* wb = wot + ((size_t)((16 * wid) * 36 + 3 * nt) << 10);
  bf16x8 fb[4][4], fa[4][3];
  auto issue = [&](int s) {
    const int ks = (s + rot) & 15;
    const char* po = ob + (size_t)ks * 65536;
    const char* pw = wb + (size_t)ks * 36864;
#pragma unroll
    for (int i = 0; i < 4; ++i) asm volatile("global_load_dwordx4 %0, %1, %2 offset:%3" : "=v"(fb[s & 3][i]) : "v"(voff), "s"(po), "i"(i * 1024) : "memory");
#pragma unroll
    for (int i = 0; i < 3; ++i) asm volatile("global_load_dwordx4 %0, %1, %2 offset:%3" : "=v"(fa[s & 3][i]) : "v"(voff), "s"(pw), "i"(i * 1024) : "memory");
  };
#pragma unroll
  for (int s = 0; s < 4; ++s) issue(s);
  f32x4 acc[4][3];
#pragma unroll
  for (int i = 0; i < 4; ++i)
#pragma unroll
    for (int c = 0; c < 3; ++c) acc[i][c] = f32x4{0.f, 0.f, 0.f, 0.f};
#pragma unroll
  for (int s = 0; s < 16; ++s) {
    const int young = 7 * ((15 - s) < 3 ? (15 - s) : 3);
    asm volatile("s_waitcnt vmcnt(%7)" : "+v"(fb[s & 3][0]), "+v"(fb[s & 3][1]), "+v"(fb[s & 3][2]), "+v"(fb[s & 3][3]), "+v"(fa[s & 3][0]), "+v"(fa[s & 3][1]), "+v"(fa[s & 3][2]) : "i"(young) : "memory");
#pragma unroll
    for (int i = 0; i < 4; ++i)
#pragma unroll
      for (int c = 0; c < 3; ++c) acc[i][c] = mfma16(fa[s & 3][c], fb[s & 3][i], acc[i][c]);
    __builtin_amdgcn_sched_barrier(0);
    if (s + 4 < 16) issue(s + 4);
    __builtin_amdgcn_sched_barrier(0);
  }
  {
    char* xs = lds + wid * 12288 + lane * 16;
#pragma unroll
    for (int i = 0; i < 4; ++i)
#pragma unroll
      for (int c = 0; c < 3; ++c) *(f32x4*)(xs + (i * 3 + c) * 1024) = acc[i][c];
  }
  __syncthreads();
#pragma unroll
  for (int it = 0; it < 2; ++it) {
    const int ci = tid + 512 * it;
    if (ci < 768) {
      f32x4 sum = *(const f32x4*)(lds + ci * 16);
#pragma unroll
      for (int w = 1; w < 8; ++w) sum += *(const f32x4*)(lds + w * 12288 + ci * 16);
      const int tl = ci >> 6, ln = ci & 63, mi = tl / 3, c = tl - mi * 3;
      u32x2 w2 = {cvtpk(sum[0], sum[1]), cvtpk(sum[2], sum[3])};
      *(u32x2*)(out + (size_t)(64 * mt + 16 * mi + (ln & 15)) * DM + 48 * nt + 16 * c + 4 * (ln >> 4)) = w2;
    }
  }
}

extern "C" void kernel_launch(void* const* d_in, const int* in_sizes, int n_in, void* d_out, int out_size, void* d_ws, size_t ws_size,
                              hipStream_t stream) {
  const u16* x = (const u16*)d_in[0];
  const u16* kc = (const u16*)d_in[1];
  const u16* vc = (const u16*)d_in[2];
  const u16* wq = (const u16*)d_in[3];
  const u16* wo = (const u16*)d_in[4];
  u16* out = (u16*)d_out;
  u16* qws = (u16*)d_ws;
  u16* ows = qws + (size_t)MROWS * QW;
  hipFuncSetAttribute((const void*)attn_kernel, hipFuncAttributeMaxDynamicSharedMemorySize, ATT_LDS);
  char* wot = (char*)d_ws + (24 << 20);
  attn_kernel<<<256, 512, ATT_LDS, stream>>>(x, wq, kc, vc, ows, wo, wot);
  hipFuncSetAttribute((const void*)oproj_kernel, hipFuncAttributeMaxDynamicSharedMemorySize, OP_LDS);
  oproj_kernel<<<192, 512, OP_LDS, stream>>>((const char*)ows, wot, out);
}
```

```cpp
#define MAPPING 0
#include <hip/hip_runtime.h>
#include <hip/hip_bf16.h>
#include <stdint.h>

typedef __attribute__((ext_vector_type(8))) short bf16x8;
typedef __attribute__((ext_vector_type(4))) short s16x4;
typedef __attribute__((ext_vector_type(16))) float f32x16;
typedef __attribute__((ext_vector_type(4))) float f32x4;
typedef __attribute__((ext_vector_type(2))) float f32x2;
typedef __attribute__((ext_vector_type(4))) unsigned u32x4;
typedef __attribute__((ext_vector_type(2))) unsigned u32x2;
typedef __attribute__((ext_vector_type(2))) __bf16 bf16x2_t;
typedef unsigned short u16;
typedef __attribute__((address_space(3))) s16x4* lds_s16x4_p;

#define DI __device__ __forceinline__

constexpr int NB = 16, SQ = 64, DM = 576, HQ = 32, HKV = 16, SK = 1024, DH = 128;
constexpr int MROWS = NB * SQ;
constexpr int QW = HQ * DH;

DI unsigned cvtpk(float lo, float hi) {
  f32x2 v = {lo, hi};
  bf16x2_t b = __builtin_convertvector(v, bf16x2_t);
  return __builtin_bit_cast(unsigned, b);
}
DI s16x4 tr_read(const char* p) {
  return __builtin_amdgcn_ds_read_tr16_b64_v4i16((lds_s16x4_p)(p));
}
DI f32x16 mfma32(bf16x8 a, bf16x8 b, f32x16 c) { return __builtin_amdgcn_mfma_f32_32x32x16_bf16(a, b, c, 0, 0, 0); }

constexpr int NS = 4;
constexpr int SLOT = 32768;
constexpr int ATT_LDS = 163840;
constexpr float SC = 0.08838834764831845f * 1.4426950408889634f;

DI void dma16(unsigned ldsaddr, unsigned voff, const void* sbase) {
  unsigned keep;
  asm volatile("s_mov_b32 %0, m0\n\ts_mov_b32 m0, %1\n\ts_nop 0\n\tglobal_load_lds_dwordx4 %2, %3 nt\n\ts_mov_b32 m0, %0"
               : "=&s"(keep) : "s"(ldsaddr), "v"(voff), "s"(sbase) : "memory");
}

DI void dma16p(unsigned ldsaddr, unsigned voff, const void* sbase) {
  unsigned keep;
  asm volatile("s_mov_b32 %0, m0\n\ts_mov_b32 m0, %1\n\ts_nop 0\n\tglobal_load_lds_dwordx4 %2, %3\n\ts_mov_b32 m0, %0"
               : "=&s"(keep) : "s"(ldsaddr), "v"(voff), "s"(sbase) : "memory");
}

struct AttnState {
  f32x16 o[4];
  f32x16 negm;
  float m, l;
};

template <int N> DI void wait_vm() { asm volatile("s_waitcnt vmcnt(%0)" ::"i"(N) : "memory"); }

struct AttnCtx {
  const char* kbase;
  const char* vbase;
  unsigned koff[2], voff[2];
  unsigned ldsw;
  unsigned kaddr[8];
  unsigned vaddr[8];
};

#define SBAR() __builtin_amdgcn_sched_barrier(0)
constexpr float THR = 8.0f;
template <int SL> DI void issue_k(const AttnCtx& c, int t) {
  const char* kb = c.kbase + (size_t)t * 16384;
  unsigned l0 = SL * 16384 + c.ldsw;
  dma16(l0, c.koff[0], kb);
  dma16(l0 + 1024, c.koff[1], kb);
}
template <int SL> DI void issue_v(const AttnCtx& c, int t) {
  const char* vb = c.vbase + (size_t)t * 16384;
  unsigned l0 = 65536 + SL * 16384 + c.ldsw;
  dma16(l0, c.voff[0], vb);
  dma16(l0 + 1024, c.voff[1], vb);
}
DI float rowmax16(const f32x16& s) {
  float pm = s[0];
#pragma unroll
  for (int i = 1; i < 16; ++i) pm = fmaxf(pm, s[i]);
  auto rr = __builtin_amdgcn_permlane32_swap(__float_as_uint(pm), __float_as_uint(pm), false, false);
  return fmaxf(__uint_as_float(rr[0]), __uint_as_float(rr[1]));
}
template <int SL, int WAITN, bool ISSUEK, bool ISSUEV, bool NEXT> DI void step(const AttnCtx& c, AttnState& st, f32x16& sc, const bf16x8* qf, const char* lds, int t) {
  wait_vm<WAITN>();
  asm volatile("s_waitcnt lgkmcnt(0)" ::: "memory");
  __builtin_amdgcn_s_barrier();
  SBAR();
  const char* Ks = lds + ((SL + 1) & 3) * 16384;
  const char* Vs = lds + SL * 16384;
  bf16x8 kf[8];
  if (NEXT) {
#pragma unroll
    for (int i = 0; i < 8; ++i) kf[i] = *(const bf16x8*)(Ks + c.kaddr[i]);
  }
  SBAR();
  if (ISSUEK) issue_k<SL>(c, t + 4);
  if (ISSUEV) issue_v<(SL + 3) & 3>(c, t + 3);
  SBAR();
  f32x16 sn;
  if (NEXT) {
    asm("v_mfma_f32_32x32x16_bf16 %0, %1, %2, %3" : "=&v"(sn) : "v"(kf[0]), "v"(qf[0]), "v"(st.negm));
#pragma unroll
    for (int i = 1; i < 8; ++i) sn = mfma32(kf[i], qf[i], sn);
  }
  s16x4 vlo[4][2], vhi[4][2];
#pragma unroll
  for (int k = 0; k < 2; ++k)
#pragma unroll
    for (int d = 0; d < 4; ++d) {
      vlo[d][k] = tr_read(Vs + c.vaddr[2 * d] + k * 4096);
      vhi[d][k] = tr_read(Vs + c.vaddr[2 * d + 1] + k * 4096);
    }
  float ps = 0.f;
#pragma unroll
  for (int i = 0; i < 16; ++i) { sc[i] = __builtin_amdgcn_exp2f(sc[i]); ps += sc[i]; }
  st.l += ps;
  bf16x8 pf[2];
#pragma unroll
  for (int k = 0; k < 2; ++k) {
    u32x4 w = {cvtpk(sc[8 * k], sc[8 * k + 1]), cvtpk(sc[8 * k + 2], sc[8 * k + 3]), cvtpk(sc[8 * k + 4], sc[8 * k + 5]), cvtpk(sc[8 * k + 6], sc[8 * k + 7])};
    pf[k] = __builtin_bit_cast(bf16x8, w);
  }
  if (NEXT) {
#pragma unroll
    for (int i = 0; i < 8; ++i) {
      __builtin_amdgcn_sched_group_barrier(0x8, 1, 0);
      __builtin_amdgcn_sched_group_barrier(0x100, 2, 0);
      __builtin_amdgcn_sched_group_barrier(0x2, 5, 0);
    }
  }
  SBAR();
#pragma unroll
  for (int k = 0; k < 2; ++k) {
#pragma unroll
    for (int d = 0; d < 4; ++d) {
      bf16x8 vf = __builtin_shufflevector(vlo[d][k], vhi[d][k], 0, 1, 2, 3, 4, 5, 6, 7);
      st.o[d] = mfma32(vf, pf[k], st.o[d]);
    }
  }
  if (NEXT) {
    const float pm = rowmax16(sn);
    SBAR();
    if (__builtin_expect(__any(pm > THR), 0)) {
      const float dl = fmaxf(pm, 0.f);
      const float alpha = __builtin_amdgcn_exp2f(-dl);
      st.m += dl;
      st.l *= alpha;
#pragma unroll
      for (int i = 0; i < 16; ++i) { sn[i] -= dl; st.negm[i] = -st.m; }
#pragma unroll
      for (int d = 0; d < 4; ++d)
#pragma unroll
        for (int i = 0; i < 16; ++i) st.o[d][i] *= alpha;
    }
    sc = sn;
  }
  SBAR();
}

__global__ __launch_bounds__(512, 2) void attn_kernel(const u16* __restrict__ x, const u16* __restrict__ wq, const u16* __restrict__ kc,
                                                      const u16* __restrict__ vc, u16* __restrict__ og,
                                                      const u16* __restrict__ wo, char* __restrict__ wot) {
  extern __shared__ __attribute__((aligned(16))) char lds[];
  const int bid = blockIdx.x;
  const int hkv = 2 * (bid & 7) + ((bid >> 3) & 1), b = bid >> 4;
  const int rot = (b * 9) >> 3;
  const int tid = threadIdx.x, lane = tid & 63, r = lane & 31, h = lane >> 5;
  const int wid = __builtin_amdgcn_readfirstlane(tid >> 6);
  const int kg = wid >> 2, g = (wid >> 1) & 1, pb = wid & 1;
  const int head = hkv * 2 + g;

  AttnCtx c;
  c.kbase = (const char*)(kc + (size_t)(b * HKV + hkv) * SK * DH);
  c.vbase = (const char*)(vc + (size_t)(b * HKV + hkv) * SK * DH);
  c.ldsw = wid * 8 * 256;
#pragma unroll
  for (int i = 0; i < 2; ++i) {
    const int row = 8 * wid + 4 * i + (lane >> 4), pos = lane & 15;
    c.koff[i] = row * 256 + ((pos ^ (row & 15)) << 4);
    c.voff[i] = row * 256 + ((pos ^ (((row & 3) << 2) | ((row >> 2) & 3))) << 4);
  }
  {
    const int krow = 32 * kg + r;
#pragma unroll
    for (int i = 0; i < 8; ++i) c.kaddr[i] = krow * 256 + (((2 * i + h) ^ (r & 15)) << 4);
    const int blk = (lane >> 4) & 1, q4 = (lane & 15) >> 2, p4 = lane & 3;
#pragma unroll
    for (int d = 0; d < 4; ++d)
#pragma unroll
      for (int t = 0; t < 2; ++t) {
        const int row = 32 * kg + 8 * t + 4 * h + q4;
        const int ch = 4 * d + 2 * blk + (p4 >> 1);
        const int sw = ((row & 3) << 2) | ((row >> 2) & 3);
        c.vaddr[2 * d + t] = 65536 + row * 256 + ((ch ^ sw) << 4) + 8 * (p4 & 1);
        asm volatile("" : "+v"(c.vaddr[2 * d + t]));
      }
  }
  const char* wqb = (const char*)(wq + (size_t)hkv * 256);
  const char* xb = (const char*)(x + (size_t)b * SQ * DM);
  constexpr unsigned XB0 = 131072;
  u32x4 wl[8];
  const int wk = bid >> 1, wgi = (bid & 1) * 36 + wid * 16 + (lane & 15);
  const bool wact = wid < 3 && (wid * 16 + (lane & 15)) < 36;
  {
    const int gcl = wact ? wgi : (bid & 1) * 36;
    const u16* wp = wo + (size_t)(32 * wk + 8 * (lane >> 4)) * DM + gcl * 8;
#pragma unroll
    for (int j = 0; j < 8; ++j) asm volatile("global_load_dwordx4 %0, %1, off nt" : "=v"(wl[j]) : "v"(wp + (size_t)j * DM) : "memory");
  }
  unsigned wsrc[2], xsrc;
#pragma unroll
  for (int i = 0; i < 2; ++i) {
    const int row = 4 * wid + 2 * i + (lane >> 5), pos = lane & 31;
    const int f = ((row & 3) << 2) | ((row >> 2) & 3);
    wsrc[i] = row * 8192 + ((pos ^ f) << 4);
  }
  {
    const int row = 16 * (wid & 3) + (lane >> 2), pos = lane & 3;
    xsrc = row * 1152 + ((pos ^ ((row >> 2) & 3)) << 4);
  }
  auto issue_chunk = [&](int chi, unsigned wbuf, unsigned xbuf) {
    int ch = chi + rot; if (ch >= 18) ch -= 18;
    const char* wsb = wqb + (size_t)ch * 32 * 8192;
#pragma unroll
    for (int i = 0; i < 2; ++i) dma16p(wbuf + wid * 2048 + i * 1024, wsrc[i], wsb);
    if (wid < 4) dma16p(xbuf + wid * 1024, xsrc, xb + ch * 64);
  };
  constexpr unsigned PS[8] = {6, 7, 0, 1, 4, 2, 5, 3};
#pragma unroll
  for (int ci = 0; ci < 8; ++ci) issue_chunk(ci, PS[ci] * 16384, XB0 + ci * 4096);
  unsigned waddr[2][2], xaddr[2];
  {
    const int blk = (lane >> 4) & 1, q4 = (lane & 15) >> 2, p4 = lane & 3;
#pragma unroll
    for (int j = 0; j < 2; ++j)
#pragma unroll
      for (int t = 0; t < 2; ++t) {
        const int row = 8 * h + 4 * t + q4;
        const int chn = 4 * (4 * g + 2 * kg + j) + 2 * blk + (p4 >> 1);
        const int f = ((row & 3) << 2) | ((row >> 2) & 3);
        waddr[j][t] = row * 512 + ((chn ^ f) << 4) + 8 * (p4 & 1);
      }
    const int row = 32 * pb + r;
#pragma unroll
    for (int s = 0; s < 2; ++s) xaddr[s] = row * 64 + (((2 * s + h) ^ ((row >> 2) & 3)) << 4);
  }
  f32x16 qa[2] = {};
  auto proj_chunk = [&](unsigned wbuf, unsigned xbuf) {
#pragma unroll
    for (int s = 0; s < 2; ++s) {
      const bf16x8 xf = *(const bf16x8*)(lds + xbuf + xaddr[s]);
#pragma unroll
      for (int j = 0; j < 2; ++j) {
        s16x4 lo = tr_read(lds + wbuf + s * 8192 + waddr[j][0]);
        s16x4 hi = tr_read(lds + wbuf + s * 8192 + waddr[j][1]);
        qa[j] = mfma32(__builtin_shufflevector(lo, hi, 0, 1, 2, 3, 4, 5, 6, 7), xf, qa[j]);
      }
    }
  };
#pragma unroll
  for (int it = 0; it < 9; ++it) {
    {
      const int yc = it == 0 ? 6 : ((2 * it + 5 <= 17 ? 2 * it + 5 : 17) - (2 * it + 1));
      const int yt = it > 6 ? 2 * (it - 6) : 0;
      if (wid < 4) asm volatile("s_waitcnt vmcnt(%0)" ::"i"(3 * yc + 2 * yt) : "memory");
      else asm volatile("s_waitcnt vmcnt(%0)" ::"i"(2 * yc + 2 * yt) : "memory");
    }
    asm volatile("s_waitcnt lgkmcnt(0)" ::: "memory");
    __builtin_amdgcn_s_barrier();
    if (it >= 1 && it <= 5) {
      issue_chunk(2 * it + 6, PS[(2 * it + 6) & 7] * 16384, XB0 + ((2 * it + 6) & 7) * 4096);
      issue_chunk(2 * it + 7, PS[(2 * it + 7) & 7] * 16384, XB0 + ((2 * it + 7) & 7) * 4096);
    }
    if (it == 6) { issue_k<0>(c, 0); issue_k<1>(c, 1); }
    if (it == 7) { issue_v<0>(c, 0); issue_k<2>(c, 2); }
    if (it == 8) { issue_v<1>(c, 1); issue_k<3>(c, 3); }
    proj_chunk(PS[(2 * it) & 7] * 16384, XB0 + ((2 * it) & 7) * 4096);
    proj_chunk(PS[(2 * it + 1) & 7] * 16384, XB0 + ((2 * it + 1) & 7) * 4096);
    if (it == 4) {
  asm volatile("" : "+v"(wl[0]), "+v"(wl[1]), "+v"(wl[2]), "+v"(wl[3]), "+v"(wl[4]), "+v"(wl[5]), "+v"(wl[6]), "+v"(wl[7]));
  if (wid < 3) {
    const int ntile = wgi >> 1;
    char* dstb = wot + ((size_t)(wk * 36 + ntile) << 10) + ((lane >> 4) * 16 + (wgi & 1) * 8) * 16;
#pragma unroll
    for (int e2 = 0; e2 < 4; ++e2) {
      u32x4 lo, hi;
#pragma unroll
      for (int j2 = 0; j2 < 4; ++j2) {
        const unsigned a = wl[2 * j2][e2], bb = wl[2 * j2 + 1][e2];
        lo[j2] = __builtin_amdgcn_perm(bb, a, 0x05040100u);
        hi[j2] = __builtin_amdgcn_perm(bb, a, 0x07060302u);
      }
      if (wact) { *(u32x4*)(dstb + (2 * e2) * 16) = lo; *(u32x4*)(dstb + (2 * e2 + 1) * 16) = hi; }
    }
  }
    }
  }
  asm volatile("s_waitcnt lgkmcnt(0)" ::: "memory");
  __builtin_amdgcn_s_barrier();
  {
    char* qt = lds + 131072 + (g * 2 + pb) * 8192 + r * 256;
#pragma unroll
    for (int j = 0; j < 2; ++j)
#pragma unroll
      for (int a = 0; a < 4; ++a) {
        unsigned w0 = cvtpk(qa[j][4 * a], qa[j][4 * a + 1]), w1 = cvtpk(qa[j][4 * a + 2], qa[j][4 * a + 3]);
        w0 = cvtpk(__uint_as_float(w0 << 16) * SC, __uint_as_float(w0 & 0xffff0000u) * SC);
        w1 = cvtpk(__uint_as_float(w1 << 16) * SC, __uint_as_float(w1 & 0xffff0000u) * SC);
        u32x2 w = {w0, w1};
        *(u32x2*)(qt + (((8 * kg + 4 * j + a) ^ (r & 15)) << 4) + 8 * h) = w;
      }
  }
  issue_v<2>(c, 2);
  wait_vm<12>();
  asm volatile("s_waitcnt lgkmcnt(0)" ::: "memory");
  __builtin_amdgcn_s_barrier();
  bf16x8 qf[8];
  {
    const char* qt = lds + 131072 + (g * 2 + pb) * 8192 + r * 256;
#pragma unroll
    for (int i = 0; i < 8; ++i) qf[i] = *(const bf16x8*)(qt + (((2 * i + h) ^ (r & 15)) << 4));
  }

  AttnState st;
#pragma unroll
  for (int d = 0; d < 4; ++d) st.o[d] = f32x16{};
  st.m = -1e30f; st.l = 0.f;

  f32x16 sc = {};
#pragma unroll
  for (int i = 0; i < 8; ++i) sc = mfma32(*(const bf16x8*)(lds + c.kaddr[i]), qf[i], sc);
  st.m = rowmax16(sc);
#pragma unroll
  for (int i = 0; i < 16; ++i) { sc[i] -= st.m; st.negm[i] = -st.m; }
  step<0, 8, true, true, true>(c, st, sc, qf, lds, 0);
  step<1, 8, true, true, true>(c, st, sc, qf, lds, 1);
  step<2, 8, true, true, true>(c, st, sc, qf, lds, 2);
  step<3, 8, true, true, true>(c, st, sc, qf, lds, 3);
#pragma unroll 1
  for (int t = 4; t < 12; t += 4) {
    step<0, 8, true, true, true>(c, st, sc, qf, lds, t);
    step<1, 8, true, true, true>(c, st, sc, qf, lds, t + 1);
    step<2, 8, true, true, true>(c, st, sc, qf, lds, t + 2);
    step<3, 8, true, true, true>(c, st, sc, qf, lds, t + 3);
  }
  step<0, 8, false, true, true>(c, st, sc, qf, lds, 12);
  step<1, 6, false, false, true>(c, st, sc, qf, lds, 13);
  step<2, 2, false, false, true>(c, st, sc, qf, lds, 14);
  step<3, 0, false, false, false>(c, st, sc, qf, lds, 15);

  float lt = st.l + __shfl_xor(st.l, 32);
  __syncthreads();
  char* xw = lds + wid * 8704;
  char* xp = lds + (wid ^ 4) * 8704;
  {
#pragma unroll
    for (int dd = 0; dd < 2; ++dd) {
      const f32x16 v = kg ? st.o[dd] : st.o[2 + dd];
#pragma unroll
      for (int a = 0; a < 4; ++a) {
        f32x4 w = {v[4 * a], v[4 * a + 1], v[4 * a + 2], v[4 * a + 3]};
        *(f32x4*)(xw + ((dd * 4 + a) * 64 + lane) * 16) = w;
      }
    }
    f32x2 ml = {st.m, lt};
    *(f32x2*)(xw + 8192 + lane * 8) = ml;
  }
  __syncthreads();
  f32x2 pml = *(const f32x2*)(xp + 8192 + lane * 8);
  const float mm = fmaxf(st.m, pml[0]);
  const float fa = __builtin_amdgcn_exp2f(st.m - mm), fb = __builtin_amdgcn_exp2f(pml[0] - mm);
  const float inv = 1.0f / (lt * fa + pml[1] * fb);
  const float wa = fa * inv, wb = fb * inv;
  char* stg = lds + 8 * 8704 + wid * 4608;
#pragma unroll
  for (int dd = 0; dd < 2; ++dd) {
    const f32x16 own = kg ? st.o[2 + dd] : st.o[dd];
#pragma unroll
    for (int a = 0; a < 4; ++a) {
      f32x4 pv = *(const f32x4*)(xp + ((dd * 4 + a) * 64 + lane) * 16);
      float o0 = own[4 * a] * wa + pv[0] * wb, o1 = own[4 * a + 1] * wa + pv[1] * wb;
      float o2 = own[4 * a + 2] * wa + pv[2] * wb, o3 = own[4 * a + 3] * wa + pv[3] * wb;
      u32x2 w = {cvtpk(o0, o1), cvtpk(o2, o3)};
      *(u32x2*)(stg + r * 144 + (32 * dd + 8 * a + 4 * h) * 2) = w;
    }
  }
  asm volatile("s_waitcnt lgkmcnt(0)" ::: "memory");
  __builtin_amdgcn_wave_barrier();
  {
    const int kst = (head * DH + kg * 64) >> 5;
    const int mt0 = (b * SQ + 32 * pb) >> 4;
#pragma unroll
    for (int i = 0; i < 4; ++i) {
      const int mi = i >> 1, ki = i & 1;
      u32x4 w = *(const u32x4*)(stg + (16 * mi + (lane & 15)) * 144 + ki * 64 + (lane >> 4) * 16);
      char* dst = (char*)og + ((size_t)((kst + ki) * 64 + mt0 + mi) << 10) + lane * 16;
      *(u32x4*)dst = w;
    }
  }
}

DI f32x4 mfma16(bf16x8 a, bf16x8 b, f32x4 c) { return __builtin_amdgcn_mfma_f32_16x16x32_bf16(a, b, c, 0, 0, 0); }
constexpr int OP_LDS = 98304;
__global__ __launch_bounds__(512, 2) void oproj_kernel(const char* __restrict__ og, const char* __restrict__ wot, u16* __restrict__ out) {
  extern __shared__ __attribute__((aligned(16))) char lds[];
  const int bid = blockIdx.x;
  const int xc = bid & 7, j = bid >> 3;
  const int mt = (xc & 3) * 4 + (j & 3), nt = (xc >> 2) * 6 + (j >> 2);
  const int tid = threadIdx.x, lane = tid & 63;
  const int wid = __builtin_amdgcn_readfirstlane(tid >> 6);
  const int rot = (5 * j) & 15;
  const unsigned voff = lane * 16;
  const char* ob = og + ((size_t)((16 * wid) * 64 + 4 * mt) << 10);
  const char* wb = wot + ((size_t)((16 * wid) * 36 + 3 * nt) << 10);
  bf16x8 fb[4][4], fa[4][3];
  auto issue = [&](int s) {
    const int ks = (s + rot) & 15;
    const char* po = ob + (size_t)ks * 65536;
    const char* pw = wb + (size_t)ks * 36864;
#pragma unroll
    for (int i = 0; i < 4; ++i) asm volatile("global_load_dwordx4 %0, %1, %2 offset:%3" : "=v"(fb[s & 3][i]) : "v"(voff), "s"(po), "i"(i * 1024) : "memory");
#pragma unroll
    for (int i = 0; i < 3; ++i) asm volatile("global_load_dwordx4 %0, %1, %2 offset:%3" : "=v"(fa[s & 3][i]) : "v"(voff), "s"(pw), "i"(i * 1024) : "memory");
  };
#pragma unroll
  for (int s = 0; s < 4; ++s) issue(s);
  f32x4 acc[4][3];
#pragma unroll
  for (int i = 0; i < 4; ++i)
#pragma unroll
    for (int c = 0; c < 3; ++c) acc[i][c] = f32x4{0.f, 0.f, 0.f, 0.f};
#pragma unroll
  for (int s = 0; s < 16; ++s) {
    const int young = 7 * ((15 - s) < 3 ? (15 - s) : 3);
    asm volatile("s_waitcnt vmcnt(%7)" : "+v"(fb[s & 3][0]), "+v"(fb[s & 3][1]), "+v"(fb[s & 3][2]), "+v"(fb[s & 3][3]), "+v"(fa[s & 3][0]), "+v"(fa[s & 3][1]), "+v"(fa[s & 3][2]) : "i"(young) : "memory");
#pragma unroll
    for (int i = 0; i < 4; ++i)
#pragma unroll
      for (int c = 0; c < 3; ++c) acc[i][c] = mfma16(fa[s & 3][c], fb[s & 3][i], acc[i][c]);
    __builtin_amdgcn_sched_barrier(0);
    if (s + 4 < 16) issue(s + 4);
    __builtin_amdgcn_sched_barrier(0);
  }
  {
    char* xs = lds + wid * 12288 + lane * 16;
#pragma unroll
    for (int i = 0; i < 4; ++i)
#pragma unroll
      for (int c = 0; c < 3; ++c) *(f32x4*)(xs + (i * 3 + c) * 1024) = acc[i][c];
  }
  __syncthreads();
#pragma unroll
  for (int it = 0; it < 2; ++it) {
    const int ci = tid + 512 * it;
    if (ci < 768) {
      f32x4 sum = *(const f32x4*)(lds + ci * 16);
#pragma unroll
      for (int w = 1; w < 8; ++w) sum += *(const f32x4*)(lds + w * 12288 + ci * 16);
      const int tl = ci >> 6, ln = ci & 63, mi = tl / 3, c = tl - mi * 3;
      u32x2 w2 = {cvtpk(sum[0], sum[1]), cvtpk(sum[2], sum[3])};
      *(u32x2*)(out + (size_t)(64 * mt + 16 * mi + (ln & 15)) * DM + 48 * nt + 16 * c + 4 * (ln >> 4)) = w2;
    }
  }
}

extern "C" void kernel_launch(void* const* d_in, const int* in_sizes, int n_in, void* d_out, int out_size, void* d_ws, size_t ws_size,
                              hipStream_t stream) {
  const u16* x = (const u16*)d_in[0];
  const u16* kc = (const u16*)d_in[1];
  const u16* vc = (const u16*)d_in[2];
  const u16* wq = (const u16*)d_in[3];
  const u16* wo = (const u16*)d_in[4];
  u16* out = (u16*)d_out;
  u16* qws = (u16*)d_ws;
  u16* ows = qws + (size_t)MROWS * QW;
  hipFuncSetAttribute((const void*)attn_kernel, hipFuncAttributeMaxDynamicSharedMemorySize, ATT_LDS);
  char* wot = (char*)d_ws + (24 << 20);
  attn_kernel<<<256, 512, ATT_LDS, stream>>>(x, wq, kc, vc, ows, wo, wot);
  hipFuncSetAttribute((const void*)oproj_kernel, hipFuncAttributeMaxDynamicSharedMemorySize, OP_LDS);
  oproj_kernel<<<192, 512, OP_LDS, stream>>>((const char*)ows, wot, out);
}
```

```cpp
#define MAPPING 0
#include <hip/hip_runtime.h>
#include <hip/hip_bf16.h>
#include <stdint.h>

typedef __attribute__((ext_vector_type(8))) short bf16x8;
typedef __attribute__((ext_vector_type(4))) short s16x4;
typedef __attribute__((ext_vector_type(16))) float f32x16;
typedef __attribute__((ext_vector_type(4))) float f32x4;
typedef __attribute__((ext_vector_type(2))) float f32x2;
typedef __attribute__((ext_vector_type(4))) unsigned u32x4;
typedef __attribute__((ext_vector_type(2))) unsigned u32x2;
typedef __attribute__((ext_vector_type(2))) __bf16 bf16x2_t;
typedef unsigned short u16;
typedef __attribute__((address_space(3))) s16x4* lds_s16x4_p;

#define DI __device__ __forceinline__

constexpr int NB = 16, SQ = 64, DM = 576, HQ = 32, HKV = 16, SK = 1024, DH = 128;
constexpr int MROWS = NB * SQ;
constexpr int QW = HQ * DH;

DI unsigned cvtpk(float lo, float hi) {
  f32x2 v = {lo, hi};
  bf16x2_t b = __builtin_convertvector(v, bf16x2_t);
  return __builtin_bit_cast(unsigned, b);
}
DI s16x4 tr_read(const char* p) {
  return __builtin_amdgcn_ds_read_tr16_b64_v4i16((lds_s16x4_p)(p));
}
DI f32x16 mfma32(bf16x8 a, bf16x8 b, f32x16 c) { return __builtin_amdgcn_mfma_f32_32x32x16_bf16(a, b, c, 0, 0, 0); }

constexpr int NS = 4;
constexpr int SLOT = 32768;
constexpr int ATT_LDS = 163840;
constexpr float SC = 0.08838834764831845f * 1.4426950408889634f;

DI void dma16(unsigned ldsaddr, unsigned voff, const void* sbase) {
  unsigned keep;
  asm volatile("s_mov_b32 %0, m0\n\ts_mov_b32 m0, %1\n\ts_nop 0\n\tglobal_load_lds_dwordx4 %2, %3 nt\n\ts_mov_b32 m0, %0"
               : "=&s"(keep) : "s"(ldsaddr), "v"(voff), "s"(sbase) : "memory");
}

DI void dma16p(unsigned ldsaddr, unsigned voff, const void* sbase) {
  unsigned keep;
  asm volatile("s_mov_b32 %0, m0\n\ts_mov_b32 m0, %1\n\ts_nop 0\n\tglobal_load_lds_dwordx4 %2, %3\n\ts_mov_b32 m0, %0"
               : "=&s"(keep) : "s"(ldsaddr), "v"(voff), "s"(sbase) : "memory");
}

struct AttnState {
  f32x16 o[4];
  f32x16 negm;
  float m, l;
};

template <int N> DI void wait_vm() { asm volatile("s_waitcnt vmcnt(%0)" ::"i"(N) : "memory"); }

struct AttnCtx {
  const char* kbase;
  const char* vbase;
  unsigned koff[2], voff[2];
  unsigned ldsw;
  unsigned kaddr[8];
  unsigned vaddr[8];
};

#define SBAR() __builtin_amdgcn_sched_barrier(0)
constexpr float THR = 8.0f;
template <int SL> DI void issue_k(const AttnCtx& c, int t) {
  const char* kb = c.kbase + (size_t)t * 16384;
  unsigned l0 = SL * 16384 + c.ldsw;
  dma16(l0, c.koff[0], kb);
  dma16(l0 + 1024, c.koff[1], kb);
}
template <int SL> DI void issue_v(const AttnCtx& c, int t) {
  const char* vb = c.vbase + (size_t)t * 16384;
  unsigned l0 = 65536 + SL * 16384 + c.ldsw;
  dma16(l0, c.voff[0], vb);
  dma16(l0 + 1024, c.voff[1], vb);
}
DI float rowmax16(const f32x16& s) {
  float pm = s[0];
#pragma unroll
  for (int i = 1; i < 16; ++i) pm = fmaxf(pm, s[i]);
  auto rr = __builtin_amdgcn_permlane32_swap(__float_as_uint(pm), __float_as_uint(pm), false, false);
  return fmaxf(__uint_as_float(rr[0]), __uint_as_float(rr[1]));
}
template <int SL, int WAITN, bool ISSUEK, bool ISSUEV, bool NEXT> DI void step(const AttnCtx& c, AttnState& st, f32x16& sc, const bf16x8* qf, const char* lds, int t) {
  wait_vm<WAITN>();
  asm volatile("s_waitcnt lgkmcnt(0)" ::: "memory");
  __builtin_amdgcn_s_barrier();
  SBAR();
  const char* Ks = lds + ((SL + 1) & 3) * 16384;
  const char* Vs = lds + SL * 16384;
  bf16x8 kf[8];
  if (NEXT) {
#pragma unroll
    for (int i = 0; i < 8; ++i) kf[i] = *(const bf16x8*)(Ks + c.kaddr[i]);
  }
  SBAR();
  if (ISSUEK) issue_k<SL>(c, t + 4);
  if (ISSUEV) issue_v<(SL + 3) & 3>(c, t + 3);
  SBAR();
  f32x16 sn;
  if (NEXT) {
    asm("v_mfma_f32_32x32x16_bf16 %0, %1, %2, %3" : "=&v"(sn) : "v"(kf[0]), "v"(qf[0]), "v"(st.negm));
#pragma unroll
    for (int i = 1; i < 8; ++i) sn = mfma32(kf[i], qf[i], sn);
  }
  s16x4 vlo[4][2], vhi[4][2];
#pragma unroll
  for (int k = 0; k < 2; ++k)
#pragma unroll
    for (int d = 0; d < 4; ++d) {
      vlo[d][k] = tr_read(Vs + c.vaddr[2 * d] + k * 4096);
      vhi[d][k] = tr_read(Vs + c.vaddr[2 * d + 1] + k * 4096);
    }
  float ps = 0.f;
#pragma unroll
  for (int i = 0; i < 16; ++i) { sc[i] = __builtin_amdgcn_exp2f(sc[i]); ps += sc[i]; }
  st.l += ps;
  bf16x8 pf[2];
#pragma unroll
  for (int k = 0; k < 2; ++k) {
    u32x4 w = {cvtpk(sc[8 * k], sc[8 * k + 1]), cvtpk(sc[8 * k + 2], sc[8 * k + 3]), cvtpk(sc[8 * k + 4], sc[8 * k + 5]), cvtpk(sc[8 * k + 6], sc[8 * k + 7])};
    pf[k] = __builtin_bit_cast(bf16x8, w);
  }
  if (NEXT) {
#pragma unroll
    for (int i = 0; i < 8; ++i) {
      __builtin_amdgcn_sched_group_barrier(0x8, 1, 0);
      __builtin_amdgcn_sched_group_barrier(0x100, 2, 0);
      __builtin_amdgcn_sched_group_barrier(0x2, 5, 0);
    }
  }
  SBAR();
#pragma unroll
  for (int k = 0; k < 2; ++k) {
#pragma unroll
    for (int d = 0; d < 4; ++d) {
      bf16x8 vf = __builtin_shufflevector(vlo[d][k], vhi[d][k], 0, 1, 2, 3, 4, 5, 6, 7);
      st.o[d] = mfma32(vf, pf[k], st.o[d]);
    }
  }
  if (NEXT) {
    const float pm = rowmax16(sn);
    SBAR();
    if (__builtin_expect(__any(pm > THR), 0)) {
      const float dl = fmaxf(pm, 0.f);
      const float alpha = __builtin_amdgcn_exp2f(-dl);
      st.m += dl;
      st.l *= alpha;
#pragma unroll
      for (int i = 0; i < 16; ++i) { sn[i] -= dl; st.negm[i] = -st.m; }
#pragma unroll
      for (int d = 0; d < 4; ++d)
#pragma unroll
        for (int i = 0; i < 16; ++i) st.o[d][i] *= alpha;
    }
    sc = sn;
  }
  SBAR();
}

__global__ __launch_bounds__(512, 2) void attn_kernel(const u16* __restrict__ x, const u16* __restrict__ wq, const u16* __restrict__ kc,
                                                      const u16* __restrict__ vc, u16* __restrict__ og,
                                                      const u16* __restrict__ wo, char* __restrict__ wot) {
  extern __shared__ __attribute__((aligned(16))) char lds[];
  const int bid = blockIdx.x;
  const int hkv = 2 * (bid & 7) + ((bid >> 3) & 1), b = bid >> 4;
  const int rot = (b * 9) >> 3;
  const int tid = threadIdx.x, lane = tid & 63, r = lane & 31, h = lane >> 5;
  const int wid = __builtin_amdgcn_readfirstlane(tid >> 6);
  const int kg = wid >> 2, g = (wid >> 1) & 1, pb = wid & 1;
  const int head = hkv * 2 + g;

  AttnCtx c;
  c.kbase = (const char*)(kc + (size_t)(b * HKV + hkv) * SK * DH);
  c.vbase = (const char*)(vc + (size_t)(b * HKV + hkv) * SK * DH);
  c.ldsw = wid * 8 * 256;
#pragma unroll
  for (int i = 0; i < 2; ++i) {
    const int row = 8 * wid + 4 * i + (lane >> 4), pos = lane & 15;
    c.koff[i] = row * 256 + ((pos ^ (row & 15)) << 4);
    c.voff[i] = row * 256 + ((pos ^ (((row & 3) << 2) | ((row >> 2) & 3))) << 4);
  }
  {
    const int krow = 32 * kg + r;
#pragma unroll
    for (int i = 0; i < 8; ++i) c.kaddr[i] = krow * 256 + (((2 * i + h) ^ (r & 15)) << 4);
    const int blk = (lane >> 4) & 1, q4 = (lane & 15) >> 2, p4 = lane & 3;
#pragma unroll
    for (int d = 0; d < 4; ++d)
#pragma unroll
      for (int t = 0; t < 2; ++t) {
        const int row = 32 * kg + 8 * t + 4 * h + q4;
        const int ch = 4 * d + 2 * blk + (p4 >> 1);
        const int sw = ((row & 3) << 2) | ((row >> 2) & 3);
        c.vaddr[2 * d + t] = 65536 + row * 256 + ((ch ^ sw) << 4) + 8 * (p4 & 1);
        asm volatile("" : "+v"(c.vaddr[2 * d + t]));
      }
  }
  const char* wqb = (const char*)(wq + (size_t)hkv * 256);
  const char* xb = (const char*)(x + (size_t)b * SQ * DM);
  constexpr unsigned XB0 = 131072;
  unsigned wsrc[2], xsrc;
#pragma unroll
  for (int i = 0; i < 2; ++i) {
    const int row = 4 * wid + 2 * i + (lane >> 5), pos = lane & 31;
    const int f = ((row & 3) << 2) | ((row >> 2) & 3);
    wsrc[i] = row * 8192 + ((pos ^ f) << 4);
  }
  {
    const int row = 16 * (wid & 3) + (lane >> 2), pos = lane & 3;
    xsrc = row * 1152 + ((pos ^ ((row >> 2) & 3)) << 4);
  }
  auto issue_chunk = [&](int chi, unsigned wbuf, unsigned xbuf) {
    int ch = chi + rot; if (ch >= 18) ch -= 18;
    const char* wsb = wqb + (size_t)ch * 32 * 8192;
#pragma unroll
    for (int i = 0; i < 2; ++i) dma16p(wbuf + wid * 2048 + i * 1024, wsrc[i], wsb);
    if (wid < 4) dma16p(xbuf + wid * 1024, xsrc, xb + ch * 64);
  };
  constexpr unsigned PS[8] = {6, 7, 0, 1, 4, 2, 5, 3};
#pragma unroll
  for (int ci = 0; ci < 8; ++ci) issue_chunk(ci, PS[ci] * 16384, XB0 + ci * 4096);
  unsigned waddr[2][2], xaddr[2];
  {
    const int blk = (lane >> 4) & 1, q4 = (lane & 15) >> 2, p4 = lane & 3;
#pragma unroll
    for (int j = 0; j < 2; ++j)
#pragma unroll
      for (int t = 0; t < 2; ++t) {
        const int row = 8 * h + 4 * t + q4;
        const int chn = 4 * (4 * g + 2 * kg + j) + 2 * blk + (p4 >> 1);
        const int f = ((row & 3) << 2) | ((row >> 2) & 3);
        waddr[j][t] = row * 512 + ((chn ^ f) << 4) + 8 * (p4 & 1);
      }
    const int row = 32 * pb + r;
#pragma unroll
    for (int s = 0; s < 2; ++s) xaddr[s] = row * 64 + (((2 * s + h) ^ ((row >> 2) & 3)) << 4);
  }
  f32x16 qa[2] = {};
  auto proj_chunk = [&](unsigned wbuf, unsigned xbuf) {
#pragma unroll
    for (int s = 0; s < 2; ++s) {
      const bf16x8 xf = *(const bf16x8*)(lds + xbuf + xaddr[s]);
#pragma unroll
      for (int j = 0; j < 2; ++j) {
        s16x4 lo = tr_read(lds + wbuf + s * 8192 + waddr[j][0]);
        s16x4 hi = tr_read(lds + wbuf + s * 8192 + waddr[j][1]);
        qa[j] = mfma32(__builtin_shufflevector(lo, hi, 0, 1, 2, 3, 4, 5, 6, 7), xf, qa[j]);
      }
    }
  };
#pragma unroll
  for (int ch = 0; ch < 18; ++ch) {
    {
      const int yc = ch == 0 ? 7 : ((ch + 6 <= 17 ? ch + 6 : 17) - ch);
      const int yt = ch > 11 ? ch - 11 : 0;
      if (wid < 4) asm volatile("s_waitcnt vmcnt(%0)" ::"i"(3 * yc + 2 * yt) : "memory");
      else asm volatile("s_waitcnt vmcnt(%0)" ::"i"(2 * yc + 2 * yt) : "memory");
    }
    asm volatile("s_waitcnt lgkmcnt(0)" ::: "memory");
    __builtin_amdgcn_s_barrier();
    if (ch >= 1 && ch <= 10) issue_chunk(ch + 7, PS[(ch + 7) & 7] * 16384, XB0 + ((ch + 7) & 7) * 4096);
    if (ch == 11) issue_k<0>(c, 0);
    if (ch == 12) issue_k<1>(c, 1);
    if (ch == 13) issue_v<0>(c, 0);
    if (ch == 14) issue_k<2>(c, 2);
    if (ch == 15) issue_v<1>(c, 1);
    if (ch == 16) issue_k<3>(c, 3);
    if (ch == 17) issue_v<2>(c, 2);
    proj_chunk(PS[ch & 7] * 16384, XB0 + (ch & 7) * 4096);
  }
  asm volatile("s_waitcnt lgkmcnt(0)" ::: "memory");
  __builtin_amdgcn_s_barrier();
  {
    char* qt = lds + 131072 + (g * 2 + pb) * 8192 + r * 256;
#pragma unroll
    for (int j = 0; j < 2; ++j)
#pragma unroll
      for (int a = 0; a < 4; ++a) {
        unsigned w0 = cvtpk(qa[j][4 * a], qa[j][4 * a + 1]), w1 = cvtpk(qa[j][4 * a + 2], qa[j][4 * a + 3]);
        w0 = cvtpk(__uint_as_float(w0 << 16) * SC, __uint_as_float(w0 & 0xffff0000u) * SC);
        w1 = cvtpk(__uint_as_float(w1 << 16) * SC, __uint_as_float(w1 & 0xffff0000u) * SC);
        u32x2 w = {w0, w1};
        *(u32x2*)(qt + (((8 * kg + 4 * j + a) ^ (r & 15)) << 4) + 8 * h) = w;
      }
  }
  wait_vm<12>();
  asm volatile("s_waitcnt lgkmcnt(0)" ::: "memory");
  __builtin_amdgcn_s_barrier();
  bf16x8 qf[8];
  {
    const char* qt = lds + 131072 + (g * 2 + pb) * 8192 + r * 256;
#pragma unroll
    for (int i = 0; i < 8; ++i) qf[i] = *(const bf16x8*)(qt + (((2 * i + h) ^ (r & 15)) << 4));
  }

  AttnState st;
#pragma unroll
  for (int d = 0; d < 4; ++d) st.o[d] = f32x16{};
  st.m = -1e30f; st.l = 0.f;

  f32x16 sc = {};
#pragma unroll
  for (int i = 0; i < 8; ++i) sc = mfma32(*(const bf16x8*)(lds + c.kaddr[i]), qf[i], sc);
  st.m = rowmax16(sc);
#pragma unroll
  for (int i = 0; i < 16; ++i) { sc[i] -= st.m; st.negm[i] = -st.m; }
  step<0, 8, true, true, true>(c, st, sc, qf, lds, 0);
  step<1, 8, true, true, true>(c, st, sc, qf, lds, 1);
  step<2, 8, true, true, true>(c, st, sc, qf, lds, 2);
  step<3, 8, true, true, true>(c, st, sc, qf, lds, 3);
#pragma unroll 1
  for (int t = 4; t < 12; t += 4) {
    step<0, 8, true, true, true>(c, st, sc, qf, lds, t);
    step<1, 8, true, true, true>(c, st, sc, qf, lds, t + 1);
    step<2, 8, true, true, true>(c, st, sc, qf, lds, t + 2);
    step<3, 8, true, true, true>(c, st, sc, qf, lds, t + 3);
  }
  step<0, 8, false, true, true>(c, st, sc, qf, lds, 12);
  step<1, 6, false, false, true>(c, st, sc, qf, lds, 13);
  step<2, 2, false, false, true>(c, st, sc, qf, lds, 14);
  step<3, 0, false, false, false>(c, st, sc, qf, lds, 15);

  u32x4 wl[8];
  const int wk = bid >> 1, wgi = (bid & 1) * 36 + wid * 16 + (lane & 15);
  const bool wact = wid < 3 && (wid * 16 + (lane & 15)) < 36;
  if (wid < 3) {
    const int gcl = wact ? wgi : (bid & 1) * 36;
    const u16* wp = wo + (size_t)(32 * wk + 8 * (lane >> 4)) * DM + gcl * 8;
#pragma unroll
    for (int j = 0; j < 8; ++j) wl[j] = __builtin_nontemporal_load((const u32x4*)(wp + (size_t)j * DM));
  }
  float lt = st.l + __shfl_xor(st.l, 32);
  __syncthreads();
  char* xw = lds + wid * 8704;
  char* xp = lds + (wid ^ 4) * 8704;
  {
#pragma unroll
    for (int dd = 0; dd < 2; ++dd) {
      const f32x16 v = kg ? st.o[dd] : st.o[2 + dd];
#pragma unroll
      for (int a = 0; a < 4; ++a) {
        f32x4 w = {v[4 * a], v[4 * a + 1], v[4 * a + 2], v[4 * a + 3]};
        *(f32x4*)(xw + ((dd * 4 + a) * 64 + lane) * 16) = w;
      }
    }
    f32x2 ml = {st.m, lt};
    *(f32x2*)(xw + 8192 + lane * 8) = ml;
  }
  __syncthreads();
  f32x2 pml = *(const f32x2*)(xp + 8192 + lane * 8);
  const float mm = fmaxf(st.m, pml[0]);
  const float fa = __builtin_amdgcn_exp2f(st.m - mm), fb = __builtin_amdgcn_exp2f(pml[0] - mm);
  const float inv = 1.0f / (lt * fa + pml[1] * fb);
  const float wa = fa * inv, wb = fb * inv;
  char* stg = lds + 8 * 8704 + wid * 4608;
#pragma unroll
  for (int dd = 0; dd < 2; ++dd) {
    const f32x16 own = kg ? st.o[2 + dd] : st.o[dd];
#pragma unroll
    for (int a = 0; a < 4; ++a) {
      f32x4 pv = *(const f32x4*)(xp + ((dd * 4 + a) * 64 + lane) * 16);
      float o0 = own[4 * a] * wa + pv[0] * wb, o1 = own[4 * a + 1] * wa + pv[1] * wb;
      float o2 = own[4 * a + 2] * wa + pv[2] * wb, o3 = own[4 * a + 3] * wa + pv[3] * wb;
      u32x2 w = {cvtpk(o0, o1), cvtpk(o2, o3)};
      *(u32x2*)(stg + r * 144 + (32 * dd + 8 * a + 4 * h) * 2) = w;
    }
  }
  asm volatile("s_waitcnt lgkmcnt(0)" ::: "memory");
  __builtin_amdgcn_wave_barrier();
  {
    const int kst = (head * DH + kg * 64) >> 5;
    const int mt0 = (b * SQ + 32 * pb) >> 4;
#pragma unroll
    for (int i = 0; i < 4; ++i) {
      const int mi = i >> 1, ki = i & 1;
      u32x4 w = *(const u32x4*)(stg + (16 * mi + (lane & 15)) * 144 + ki * 64 + (lane >> 4) * 16);
      char* dst = (char*)og + ((size_t)((kst + ki) * 64 + mt0 + mi) << 10) + lane * 16;
      *(u32x4*)dst = w;
    }
  }
  if (wid < 3) {
    const int ntile = wgi >> 1;
    char* dstb = wot + ((size_t)(wk * 36 + ntile) << 10) + ((lane >> 4) * 16 + (wgi & 1) * 8) * 16;
#pragma unroll
    for (int e2 = 0; e2 < 4; ++e2) {
      u32x4 lo, hi;
#pragma unroll
      for (int j2 = 0; j2 < 4; ++j2) {
        const unsigned a = wl[2 * j2][e2], bb = wl[2 * j2 + 1][e2];
        lo[j2] = __builtin_amdgcn_perm(bb, a, 0x05040100u);
        hi[j2] = __builtin_amdgcn_perm(bb, a, 0x07060302u);
      }
      if (wact) { *(u32x4*)(dstb + (2 * e2) * 16) = lo; *(u32x4*)(dstb + (2 * e2 + 1) * 16) = hi; }
    }
  }
}

DI f32x4 mfma16(bf16x8 a, bf16x8 b, f32x4 c) { return __builtin_amdgcn_mfma_f32_16x16x32_bf16(a, b, c, 0, 0, 0); }
constexpr int OP_LDS = 98304;
__global__ __launch_bounds__(512, 2) void oproj_kernel(const char* __restrict__ og, const char* __restrict__ wot, u16* __restrict__ out) {
  extern __shared__ __attribute__((aligned(16))) char lds[];
  const int bid = blockIdx.x;
  const int xc = bid & 7, j = bid >> 3;
  const int mt = (xc & 3) * 4 + (j & 3), nt = (xc >> 2) * 6 + (j >> 2);
  const int tid = threadIdx.x, lane = tid & 63;
  const int wid = __builtin_amdgcn_readfirstlane(tid >> 6);
  const int rot = ((j >> 2) + 3 * (j & 3)) & 7;
  const unsigned voff = lane * 16;
  const char* ob = og + ((size_t)((8 * wid) * 64 + 4 * mt) << 10);
  const char* wb = wot + ((size_t)((8 * wid) * 36 + 3 * nt) << 10);
  bf16x8 fb[4][4], fa[4][3];
  auto issue = [&](int s) {
    const int ks = (s >> 3) * 64 + ((s + rot) & 7);
    const char* po = ob + (size_t)ks * 65536;
    const char* pw = wb + (size_t)ks * 36864;
#pragma unroll
    for (int i = 0; i < 4; ++i) asm volatile("global_load_dwordx4 %0, %1, %2 offset:%3" : "=v"(fb[s & 3][i]) : "v"(voff), "s"(po), "i"(i * 1024) : "memory");
#pragma unroll
    for (int i = 0; i < 3; ++i) asm volatile("global_load_dwordx4 %0, %1, %2 offset:%3" : "=v"(fa[s & 3][i]) : "v"(voff), "s"(pw), "i"(i * 1024) : "memory");
  };
#pragma unroll
  for (int s = 0; s < 4; ++s) issue(s);
  f32x4 acc[4][3];
#pragma unroll
  for (int i = 0; i < 4; ++i)
#pragma unroll
    for (int c = 0; c < 3; ++c) acc[i][c] = f32x4{0.f, 0.f, 0.f, 0.f};
#pragma unroll
  for (int s = 0; s < 16; ++s) {
    const int young = 7 * ((15 - s) < 3 ? (15 - s) : 3);
    asm volatile("s_waitcnt vmcnt(%7)" : "+v"(fb[s & 3][0]), "+v"(fb[s & 3][1]), "+v"(fb[s & 3][2]), "+v"(fb[s & 3][3]), "+v"(fa[s & 3][0]), "+v"(fa[s & 3][1]), "+v"(fa[s & 3][2]) : "i"(young) : "memory");
#pragma unroll
    for (int i = 0; i < 4; ++i)
#pragma unroll
      for (int c = 0; c < 3; ++c) acc[i][c] = mfma16(fa[s & 3][c], fb[s & 3][i], acc[i][c]);
    __builtin_amdgcn_sched_barrier(0);
    if (s + 4 < 16) issue(s + 4);
    __builtin_amdgcn_sched_barrier(0);
  }
  {
    char* xs = lds + wid * 12288 + lane * 16;
#pragma unroll
    for (int i = 0; i < 4; ++i)
#pragma unroll
      for (int c = 0; c < 3; ++c) *(f32x4*)(xs + (i * 3 + c) * 1024) = acc[i][c];
  }
  __syncthreads();
#pragma unroll
  for (int it = 0; it < 2; ++it) {
    const int ci = tid + 512 * it;
    if (ci < 768) {
      f32x4 sum = *(const f32x4*)(lds + ci * 16);
#pragma unroll
      for (int w = 1; w < 8; ++w) sum += *(const f32x4*)(lds + w * 12288 + ci * 16);
      const int tl = ci >> 6, ln = ci & 63, mi = tl / 3, c = tl - mi * 3;
      u32x2 w2 = {cvtpk(sum[0], sum[1]), cvtpk(sum[2], sum[3])};
      *(u32x2*)(out + (size_t)(64 * mt + 16 * mi + (ln & 15)) * DM + 48 * nt + 16 * c + 4 * (ln >> 4)) = w2;
    }
  }
}

extern "C" void kernel_launch(void* const* d_in, const int* in_sizes, int n_in, void* d_out, int out_size, void* d_ws, size_t ws_size,
                              hipStream_t stream) {
  const u16* x = (const u16*)d_in[0];
  const u16* kc = (const u16*)d_in[1];
  const u16* vc = (const u16*)d_in[2];
  const u16* wq = (const u16*)d_in[3];
  const u16* wo = (const u16*)d_in[4];
  u16* out = (u16*)d_out;
  u16* qws = (u16*)d_ws;
  u16* ows = qws + (size_t)MROWS * QW;
  hipFuncSetAttribute((const void*)attn_kernel, hipFuncAttributeMaxDynamicSharedMemorySize, ATT_LDS);
  char* wot = (char*)d_ws + (24 << 20);
  attn_kernel<<<256, 512, ATT_LDS, stream>>>(x, wq, kc, vc, ows, wo, wot);
  hipFuncSetAttribute((const void*)oproj_kernel, hipFuncAttributeMaxDynamicSharedMemorySize, OP_LDS);
  oproj_kernel<<<192, 512, OP_LDS, stream>>>((const char*)ows, wot, out);
}
```

```cpp
#define MAPPING 0
#include <hip/hip_runtime.h>
#include <hip/hip_bf16.h>
#include <stdint.h>

typedef __attribute__((ext_vector_type(8))) short bf16x8;
typedef __attribute__((ext_vector_type(4))) short s16x4;
typedef __attribute__((ext_vector_type(16))) float f32x16;
typedef __attribute__((ext_vector_type(4))) float f32x4;
typedef __attribute__((ext_vector_type(2))) float f32x2;
typedef __attribute__((ext_vector_type(4))) unsigned u32x4;
typedef __attribute__((ext_vector_type(2))) unsigned u32x2;
typedef __attribute__((ext_vector_type(2))) __bf16 bf16x2_t;
typedef unsigned short u16;
typedef __attribute__((address_space(3))) s16x4* lds_s16x4_p;

#define DI __device__ __forceinline__

constexpr int NB = 16, SQ = 64, DM = 576, HQ = 32, HKV = 16, SK = 1024, DH = 128;
constexpr int MROWS = NB * SQ;
constexpr int QW = HQ * DH;

DI unsigned cvtpk(float lo, float hi) {
  f32x2 v = {lo, hi};
  bf16x2_t b = __builtin_convertvector(v, bf16x2_t);
  return __builtin_bit_cast(unsigned, b);
}
DI s16x4 tr_read(const char* p) {
  return __builtin_amdgcn_ds_read_tr16_b64_v4i16((lds_s16x4_p)(p));
}
DI f32x16 mfma32(bf16x8 a, bf16x8 b, f32x16 c) { return __builtin_amdgcn_mfma_f32_32x32x16_bf16(a, b, c, 0, 0, 0); }

constexpr int NS = 4;
constexpr int SLOT = 32768;
constexpr int ATT_LDS = 163840;
constexpr float SC = 0.08838834764831845f * 1.4426950408889634f;

DI void dma16(unsigned ldsaddr, unsigned voff, const void* sbase) {
  unsigned keep;
  asm volatile("s_mov_b32 %0, m0\n\ts_mov_b32 m0, %1\n\ts_nop 0\n\tglobal_load_lds_dwordx4 %2, %3 nt\n\ts_mov_b32 m0, %0"
               : "=&s"(keep) : "s"(ldsaddr), "v"(voff), "s"(sbase) : "memory");
}

DI void dma16p(unsigned ldsaddr, unsigned voff, const void* sbase) {
  unsigned keep;
  asm volatile("s_mov_b32 %0, m0\n\ts_mov_b32 m0, %1\n\ts_nop 0\n\tglobal_load_lds_dwordx4 %2, %3\n\ts_mov_b32 m0, %0"
               : "=&s"(keep) : "s"(ldsaddr), "v"(voff), "s"(sbase) : "memory");
}

struct AttnState {
  f32x16 o[4];
  f32x16 negm;
  float m, l;
};

template <int N> DI void wait_vm() { asm volatile("s_waitcnt vmcnt(%0)" ::"i"(N) : "memory"); }

struct AttnCtx {
  const char* kbase;
  const char* vbase;
  unsigned koff[2], voff[2];
  unsigned ldsw;
  unsigned kaddr[8];
  unsigned vaddr[8];
};

#define SBAR() __builtin_amdgcn_sched_barrier(0)
constexpr float THR = 8.0f;
template <int SL> DI void issue_k(const AttnCtx& c, int t) {
  const char* kb = c.kbase + (size_t)t * 16384;
  unsigned l0 = SL * 16384 + c.ldsw;
  dma16(l0, c.koff[0], kb);
  dma16(l0 + 1024, c.koff[1], kb);
}
template <int SL> DI void issue_v(const AttnCtx& c, int t) {
  const char* vb = c.vbase + (size_t)t * 16384;
  unsigned l0 = 65536 + SL * 16384 + c.ldsw;
  dma16(l0, c.voff[0], vb);
  dma16(l0 + 1024, c.voff[1], vb);
}
DI float rowmax16(const f32x16& s) {
  float pm = s[0];
#pragma unroll
  for (int i = 1; i < 16; ++i) pm = fmaxf(pm, s[i]);
  auto rr = __builtin_amdgcn_permlane32_swap(__float_as_uint(pm), __float_as_uint(pm), false, false);
  return fmaxf(__uint_as_float(rr[0]), __uint_as_float(rr[1]));
}
template <int SL, int WAITN, bool ISSUEK, bool ISSUEV, bool NEXT> DI void step(const AttnCtx& c, AttnState& st, f32x16& sc, const bf16x8* qf, const char* lds, int t) {
  wait_vm<WAITN>();
  asm volatile("s_waitcnt lgkmcnt(0)" ::: "memory");
  __builtin_amdgcn_s_barrier();
  SBAR();
  const char* Ks = lds + ((SL + 1) & 3) * 16384;
  const char* Vs = lds + SL * 16384;
  bf16x8 kf[8];
  if (NEXT) {
#pragma unroll
    for (int i = 0; i < 8; ++i) kf[i] = *(const bf16x8*)(Ks + c.kaddr[i]);
  }
  SBAR();
  if (ISSUEK) issue_k<SL>(c, t + 4);
  if (ISSUEV) issue_v<(SL + 3) & 3>(c, t + 3);
  SBAR();
  f32x16 sn;
  if (NEXT) {
    asm("v_mfma_f32_32x32x16_bf16 %0, %1, %2, %3" : "=&v"(sn) : "v"(kf[0]), "v"(qf[0]), "v"(st.negm));
#pragma unroll
    for (int i = 1; i < 8; ++i) sn = mfma32(kf[i], qf[i], sn);
  }
  s16x4 vlo[4][2], vhi[4][2];
#pragma unroll
  for (int k = 0; k < 2; ++k)
#pragma unroll
    for (int d = 0; d < 4; ++d) {
      vlo[d][k] = tr_read(Vs + c.vaddr[2 * d] + k * 4096);
      vhi[d][k] = tr_read(Vs + c.vaddr[2 * d + 1] + k * 4096);
    }
  float ps = 0.f;
#pragma unroll
  for (int i = 0; i < 16; ++i) { sc[i] = __builtin_amdgcn_exp2f(sc[i]); ps += sc[i]; }
  st.l += ps;
  bf16x8 pf[2];
#pragma unroll
  for (int k = 0; k < 2; ++k) {
    u32x4 w = {cvtpk(sc[8 * k], sc[8 * k + 1]), cvtpk(sc[8 * k + 2], sc[8 * k + 3]), cvtpk(sc[8 * k + 4], sc[8 * k + 5]), cvtpk(sc[8 * k + 6], sc[8 * k + 7])};
    pf[k] = __builtin_bit_cast(bf16x8, w);
  }
  if (NEXT) {
#pragma unroll
    for (int i = 0; i < 8; ++i) {
      __builtin_amdgcn_sched_group_barrier(0x8, 1, 0);
      __builtin_amdgcn_sched_group_barrier(0x100, 2, 0);
      __builtin_amdgcn_sched_group_barrier(0x2, 5, 0);
    }
  }
  SBAR();
#pragma unroll
  for (int k = 0; k < 2; ++k) {
#pragma unroll
    for (int d = 0; d < 4; ++d) {
      bf16x8 vf = __builtin_shufflevector(vlo[d][k], vhi[d][k], 0, 1, 2, 3, 4, 5, 6, 7);
      st.o[d] = mfma32(vf, pf[k], st.o[d]);
    }
  }
  if (NEXT) {
    const float pm = rowmax16(sn);
    SBAR();
    if (__builtin_expect(__any(pm > THR), 0)) {
      const float dl = fmaxf(pm, 0.f);
      const float alpha = __builtin_amdgcn_exp2f(-dl);
      st.m += dl;
      st.l *= alpha;
#pragma unroll
      for (int i = 0; i < 16; ++i) { sn[i] -= dl; st.negm[i] = -st.m; }
#pragma unroll
      for (int d = 0; d < 4; ++d)
#pragma unroll
        for (int i = 0; i < 16; ++i) st.o[d][i] *= alpha;
    }
    sc = sn;
  }
  SBAR();
}

__global__ __launch_bounds__(512, 2) void attn_kernel(const u16* __restrict__ x, const u16* __restrict__ wq, const u16* __restrict__ kc,
                                                      const u16* __restrict__ vc, u16* __restrict__ og,
                                                      const u16* __restrict__ wo, char* __restrict__ wot) {
  extern __shared__ __attribute__((aligned(16))) char lds[];
  const int bid = blockIdx.x;
  const int hkv = 2 * (bid & 7) + ((bid >> 3) & 1), b = bid >> 4;
  const int rot = (b * 9) >> 3;
  const int tid = threadIdx.x, lane = tid & 63, r = lane & 31, h = lane >> 5;
  const int wid = __builtin_amdgcn_readfirstlane(tid >> 6);
  const int kg = wid >> 2, g = (wid >> 1) & 1, pb = wid & 1;
  const int head = hkv * 2 + g;

  AttnCtx c;
  c.kbase = (const char*)(kc + (size_t)(b * HKV + hkv) * SK * DH);
  c.vbase = (const char*)(vc + (size_t)(b * HKV + hkv) * SK * DH);
  c.ldsw = wid * 8 * 256;
#pragma unroll
  for (int i = 0; i < 2; ++i) {
    const int row = 8 * wid + 4 * i + (lane >> 4), pos = lane & 15;
    c.koff[i] = row * 256 + ((pos ^ (row & 15)) << 4);
    c.voff[i] = row * 256 + ((pos ^ (((row & 3) << 2) | ((row >> 2) & 3))) << 4);
  }
  {
    const int krow = 32 * kg + r;
#pragma unroll
    for (int i = 0; i < 8; ++i) c.kaddr[i] = krow * 256 + (((2 * i + h) ^ (r & 15)) << 4);
    const int blk = (lane >> 4) & 1, q4 = (lane & 15) >> 2, p4 = lane & 3;
#pragma unroll
    for (int d = 0; d < 4; ++d)
#pragma unroll
      for (int t = 0; t < 2; ++t) {
        const int row = 32 * kg + 8 * t + 4 * h + q4;
        const int ch = 4 * d + 2 * blk + (p4 >> 1);
        const int sw = ((row & 3) << 2) | ((row >> 2) & 3);
        c.vaddr[2 * d + t] = 65536 + row * 256 + ((ch ^ sw) << 4) + 8 * (p4 & 1);
        asm volatile("" : "+v"(c.vaddr[2 * d + t]));
      }
  }
  const char* wqb = (const char*)(wq + (size_t)hkv * 256);
  const char* xb = (const char*)(x + (size_t)b * SQ * DM);
  constexpr unsigned XB0 = 131072;
  unsigned wsrc[2], xsrc;
#pragma unroll
  for (int i = 0; i < 2; ++i) {
    const int row = 4 * wid + 2 * i + (lane >> 5), pos = lane & 31;
    const int f = ((row & 3) << 2) | ((row >> 2) & 3);
    wsrc[i] = row * 8192 + ((pos ^ f) << 4);
  }
  {
    const int row = 16 * (wid & 3) + (lane >> 2), pos = lane & 3;
    xsrc = row * 1152 + ((pos ^ ((row >> 2) & 3)) << 4);
  }
  auto issue_chunk = [&](int chi, unsigned wbuf, unsigned xbuf) {
    int ch = chi + rot; if (ch >= 18) ch -= 18;
    const char* wsb = wqb + (size_t)ch * 32 * 8192;
#pragma unroll
    for (int i = 0; i < 2; ++i) dma16p(wbuf + wid * 2048 + i * 1024, wsrc[i], wsb);
    if (wid < 4) dma16p(xbuf + wid * 1024, xsrc, xb + ch * 64);
  };
  constexpr unsigned PS[8] = {6, 7, 0, 1, 4, 2, 5, 3};
#pragma unroll
  for (int ci = 0; ci < 8; ++ci) issue_chunk(ci, PS[ci] * 16384, XB0 + ci * 4096);
  unsigned waddr[2][2], xaddr[2];
  {
    const int blk = (lane >> 4) & 1, q4 = (lane & 15) >> 2, p4 = lane & 3;
#pragma unroll
    for (int j = 0; j < 2; ++j)
#pragma unroll
      for (int t = 0; t < 2; ++t) {
        const int row = 8 * h + 4 * t + q4;
        const int chn = 4 * (4 * g + 2 * kg + j) + 2 * blk + (p4 >> 1);
        const int f = ((row & 3) << 2) | ((row >> 2) & 3);
        waddr[j][t] = row * 512 + ((chn ^ f) << 4) + 8 * (p4 & 1);
      }
    const int row = 32 * pb + r;
#pragma unroll
    for (int s = 0; s < 2; ++s) xaddr[s] = row * 64 + (((2 * s + h) ^ ((row >> 2) & 3)) << 4);
  }
  f32x16 qa[2] = {};
  auto proj_chunk = [&](unsigned wbuf, unsigned xbuf) {
#pragma unroll
    for (int s = 0; s < 2; ++s) {
      const bf16x8 xf = *(const bf16x8*)(lds + xbuf + xaddr[s]);
#pragma unroll
      for (int j = 0; j < 2; ++j) {
        s16x4 lo = tr_read(lds + wbuf + s * 8192 + waddr[j][0]);
        s16x4 hi = tr_read(lds + wbuf + s * 8192 + waddr[j][1]);
        qa[j] = mfma32(__builtin_shufflevector(lo, hi, 0, 1, 2, 3, 4, 5, 6, 7), xf, qa[j]);
      }
    }
  };
#pragma unroll
  for (int ch = 0; ch < 18; ++ch) {
    {
      const int yc = ch == 0 ? 7 : ((ch + 6 <= 17 ? ch + 6 : 17) - ch);
      const int yt = ch > 11 ? ch - 11 : 0;
      if (wid < 4) asm volatile("s_waitcnt vmcnt(%0)" ::"i"(3 * yc + 2 * yt) : "memory");
      else asm volatile("s_waitcnt vmcnt(%0)" ::"i"(2 * yc + 2 * yt) : "memory");
    }
    asm volatile("s_waitcnt lgkmcnt(0)" ::: "memory");
    __builtin_amdgcn_s_barrier();
    if (ch >= 1 && ch <= 10) issue_chunk(ch + 7, PS[(ch + 7) & 7] * 16384, XB0 + ((ch + 7) & 7) * 4096);
    if (ch == 11) issue_k<0>(c, 0);
    if (ch == 12) issue_k<1>(c, 1);
    if (ch == 13) issue_v<0>(c, 0);
    if (ch == 14) issue_k<2>(c, 2);
    if (ch == 15) issue_v<1>(c, 1);
    if (ch == 16) issue_k<3>(c, 3);
    if (ch == 17) issue_v<2>(c, 2);
    proj_chunk(PS[ch & 7] * 16384, XB0 + (ch & 7) * 4096);
  }
  asm volatile("s_waitcnt lgkmcnt(0)" ::: "memory");
  __builtin_amdgcn_s_barrier();
  {
    char* qt = lds + 131072 + (g * 2 + pb) * 8192 + r * 256;
#pragma unroll
    for (int j = 0; j < 2; ++j)
#pragma unroll
      for (int a = 0; a < 4; ++a) {
        unsigned w0 = cvtpk(qa[j][4 * a], qa[j][4 * a + 1]), w1 = cvtpk(qa[j][4 * a + 2], qa[j][4 * a + 3]);
        w0 = cvtpk(__uint_as_float(w0 << 16) * SC, __uint_as_float(w0 & 0xffff0000u) * SC);
        w1 = cvtpk(__uint_as_float(w1 << 16) * SC, __uint_as_float(w1 & 0xffff0000u) * SC);
        u32x2 w = {w0, w1};
        *(u32x2*)(qt + (((8 * kg + 4 * j + a) ^ (r & 15)) << 4) + 8 * h) = w;
      }
  }
  wait_vm<12>();
  asm volatile("s_waitcnt lgkmcnt(0)" ::: "memory");
  __builtin_amdgcn_s_barrier();
  bf16x8 qf[8];
  {
    const char* qt = lds + 131072 + (g * 2 + pb) * 8192 + r * 256;
#pragma unroll
    for (int i = 0; i < 8; ++i) qf[i] = *(const bf16x8*)(qt + (((2 * i + h) ^ (r & 15)) << 4));
  }

  AttnState st;
#pragma unroll
  for (int d = 0; d < 4; ++d) st.o[d] = f32x16{};
  st.m = -1e30f; st.l = 0.f;

  f32x16 sc = {};
#pragma unroll
  for (int i = 0; i < 8; ++i) sc = mfma32(*(const bf16x8*)(lds + c.kaddr[i]), qf[i], sc);
  st.m = rowmax16(sc);
#pragma unroll
  for (int i = 0; i < 16; ++i) { sc[i] -= st.m; st.negm[i] = -st.m; }
  step<0, 8, true, true, true>(c, st, sc, qf, lds, 0);
  step<1, 8, true, true, true>(c, st, sc, qf, lds, 1);
  {
    const char* wsl = (const char*)(wo + (size_t)(32 * (bid >> 1)) * DM + 288 * (bid & 1));
#pragma unroll
    for (int i = 0; i < 3; ++i) {
      const int p = wid + 8 * i;
      const int idx = p * 64 + lane, row = idx / 36, chn = idx - row * 36;
      dma16(131072 + p * 1024, p < 18 ? (unsigned)(row * 1152 + chn * 16) : 0u, wsl);
    }
  }
  step<2, 8, true, true, true>(c, st, sc, qf, lds, 2);
  step<3, 8, true, true, true>(c, st, sc, qf, lds, 3);
#pragma unroll 1
  for (int t = 4; t < 12; t += 4) {
    step<0, 8, true, true, true>(c, st, sc, qf, lds, t);
    step<1, 8, true, true, true>(c, st, sc, qf, lds, t + 1);
    step<2, 8, true, true, true>(c, st, sc, qf, lds, t + 2);
    step<3, 8, true, true, true>(c, st, sc, qf, lds, t + 3);
  }
  step<0, 8, false, true, true>(c, st, sc, qf, lds, 12);
  step<1, 6, false, false, true>(c, st, sc, qf, lds, 13);
  step<2, 2, false, false, true>(c, st, sc, qf, lds, 14);
  step<3, 0, false, false, false>(c, st, sc, qf, lds, 15);

  float lt = st.l + __shfl_xor(st.l, 32);
  __syncthreads();
  char* xw = lds + wid * 8704;
  char* xp = lds + (wid ^ 4) * 8704;
  {
#pragma unroll
    for (int dd = 0; dd < 2; ++dd) {
      const f32x16 v = kg ? st.o[dd] : st.o[2 + dd];
#pragma unroll
      for (int a = 0; a < 4; ++a) {
        f32x4 w = {v[4 * a], v[4 * a + 1], v[4 * a + 2], v[4 * a + 3]};
        *(f32x4*)(xw + ((dd * 4 + a) * 64 + lane) * 16) = w;
      }
    }
    f32x2 ml = {st.m, lt};
    *(f32x2*)(xw + 8192 + lane * 8) = ml;
  }
  __syncthreads();
  f32x2 pml = *(const f32x2*)(xp + 8192 + lane * 8);
  const float mm = fmaxf(st.m, pml[0]);
  const float fa = __builtin_amdgcn_exp2f(st.m - mm), fb = __builtin_amdgcn_exp2f(pml[0] - mm);
  const float inv = 1.0f / (lt * fa + pml[1] * fb);
  const float wa = fa * inv, wb = fb * inv;
  char* stg = lds + 8 * 8704 + wid * 4608;
#pragma unroll
  for (int dd = 0; dd < 2; ++dd) {
    const f32x16 own = kg ? st.o[2 + dd] : st.o[dd];
#pragma unroll
    for (int a = 0; a < 4; ++a) {
      f32x4 pv = *(const f32x4*)(xp + ((dd * 4 + a) * 64 + lane) * 16);
      float o0 = own[4 * a] * wa + pv[0] * wb, o1 = own[4 * a + 1] * wa + pv[1] * wb;
      float o2 = own[4 * a + 2] * wa + pv[2] * wb, o3 = own[4 * a + 3] * wa + pv[3] * wb;
      u32x2 w = {cvtpk(o0, o1), cvtpk(o2, o3)};
      *(u32x2*)(stg + r * 144 + (32 * dd + 8 * a + 4 * h) * 2) = w;
    }
  }
  asm volatile("s_waitcnt lgkmcnt(0)" ::: "memory");
  __builtin_amdgcn_wave_barrier();
  {
    const int kst = (head * DH + kg * 64) >> 5;
    const int mt0 = (b * SQ + 32 * pb) >> 4;
#pragma unroll
    for (int i = 0; i < 4; ++i) {
      const int mi = i >> 1, ki = i & 1;
      u32x4 w = *(const u32x4*)(stg + (16 * mi + (lane & 15)) * 144 + ki * 64 + (lane >> 4) * 16);
      char* dst = (char*)og + ((size_t)((kst + ki) * 64 + mt0 + mi) << 10) + lane * 16;
      *(u32x4*)dst = w;
    }
  }
  {
    const int wk = bid >> 1;
    const int g4 = lane >> 4, q4 = (lane & 15) >> 2, p4 = lane & 3;
    const char* sb = lds + 131072 + (8 * g4 + q4) * 576 + p4 * 8;
#pragma unroll
    for (int i = 0; i < 3; ++i) {
      const int cc = wid + 8 * i;
      if (cc < 18) {
        s16x4 lo = tr_read(sb + cc * 32), hi = tr_read(sb + cc * 32 + 4 * 576);
        bf16x8 af = __builtin_shufflevector(lo, hi, 0, 1, 2, 3, 4, 5, 6, 7);
        *(bf16x8*)(wot + ((size_t)(wk * 36 + (bid & 1) * 18 + cc) << 10) + lane * 16) = af;
      }
    }
  }
}

DI f32x4 mfma16(bf16x8 a, bf16x8 b, f32x4 c) { return __builtin_amdgcn_mfma_f32_16x16x32_bf16(a, b, c, 0, 0, 0); }
constexpr int OP_LDS = 98304;
__global__ __launch_bounds__(512, 2) void oproj_kernel(const char* __restrict__ og, const char* __restrict__ wot, u16* __restrict__ out) {
  extern __shared__ __attribute__((aligned(16))) char lds[];
  const int bid = blockIdx.x;
  const int xc = bid & 7, j = bid >> 3;
  const int mt = (xc & 3) * 4 + (j & 3), nt = (xc >> 2) * 6 + (j >> 2);
  const int tid = threadIdx.x, lane = tid & 63;
  const int wid = __builtin_amdgcn_readfirstlane(tid >> 6);
  const int rot = ((j >> 2) + 3 * (j & 3)) & 7;
  const unsigned voff = lane * 16;
  const char* ob = og + ((size_t)((8 * wid) * 64 + 4 * mt) << 10);
  const char* wb = wot + ((size_t)((8 * wid) * 36 + 3 * nt) << 10);
  bf16x8 fb[4][4], fa[4][3];
  auto issue = [&](int s) {
    const int ks = (s >> 3) * 64 + ((s + rot) & 7);
    const char* po = ob + (size_t)ks * 65536;
    const char* pw = wb + (size_t)ks * 36864;
#pragma unroll
    for (int i = 0; i < 4; ++i) asm volatile("global_load_dwordx4 %0, %1, %2 offset:%3" : "=v"(fb[s & 3][i]) : "v"(voff), "s"(po), "i"(i * 1024) : "memory");
#pragma unroll
    for (int i = 0; i < 3; ++i) asm volatile("global_load_dwordx4 %0, %1, %2 offset:%3" : "=v"(fa[s & 3][i]) : "v"(voff), "s"(pw), "i"(i * 1024) : "memory");
  };
#pragma unroll
  for (int s = 0; s < 4; ++s) issue(s);
  f32x4 acc[4][3];
#pragma unroll
  for (int i = 0; i < 4; ++i)
#pragma unroll
    for (int c = 0; c < 3; ++c) acc[i][c] = f32x4{0.f, 0.f, 0.f, 0.f};
#pragma unroll
  for (int s = 0; s < 16; ++s) {
    const int young = 7 * ((15 - s) < 3 ? (15 - s) : 3);
    asm volatile("s_waitcnt vmcnt(%7)" : "+v"(fb[s & 3][0]), "+v"(fb[s & 3][1]), "+v"(fb[s & 3][2]), "+v"(fb[s & 3][3]), "+v"(fa[s & 3][0]), "+v"(fa[s & 3][1]), "+v"(fa[s & 3][2]) : "i"(young) : "memory");
#pragma unroll
    for (int i = 0; i < 4; ++i)
#pragma unroll
      for (int c = 0; c < 3; ++c) acc[i][c] = mfma16(fa[s & 3][c], fb[s & 3][i], acc[i][c]);
    __builtin_amdgcn_sched_barrier(0);
    if (s + 4 < 16) issue(s + 4);
    __builtin_amdgcn_sched_barrier(0);
  }
  {
    char* xs = lds + wid * 12288 + lane * 16;
#pragma unroll
    for (int i = 0; i < 4; ++i)
#pragma unroll
      for (int c = 0; c < 3; ++c) *(f32x4*)(xs + (i * 3 + c) * 1024) = acc[i][c];
  }
  __syncthreads();
#pragma unroll
  for (int it = 0; it < 2; ++it) {
    const int ci = tid + 512 * it;
    if (ci < 768) {
      f32x4 sum = *(const f32x4*)(lds + ci * 16);
#pragma unroll
      for (int w = 1; w < 8; ++w) sum += *(const f32x4*)(lds + w * 12288 + ci * 16);
      const int tl = ci >> 6, ln = ci & 63, mi = tl / 3, c = tl - mi * 3;
      u32x2 w2 = {cvtpk(sum[0], sum[1]), cvtpk(sum[2], sum[3])};
      *(u32x2*)(out + (size_t)(64 * mt + 16 * mi + (ln & 15)) * DM + 48 * nt + 16 * c + 4 * (ln >> 4)) = w2;
    }
  }
}

extern "C" void kernel_launch(void* const* d_in, const int* in_sizes, int n_in, void* d_out, int out_size, void* d_ws, size_t ws_size,
                              hipStream_t stream) {
  const u16* x = (const u16*)d_in[0];
  const u16* kc = (const u16*)d_in[1];
  const u16* vc = (const u16*)d_in[2];
  const u16* wq = (const u16*)d_in[3];
  const u16* wo = (const u16*)d_in[4];
  u16* out = (u16*)d_out;
  u16* qws = (u16*)d_ws;
  u16* ows = qws + (size_t)MROWS * QW;
  hipFuncSetAttribute((const void*)attn_kernel, hipFuncAttributeMaxDynamicSharedMemorySize, ATT_LDS);
  char* wot = (char*)d_ws + (24 << 20);
  attn_kernel<<<256, 512, ATT_LDS, stream>>>(x, wq, kc, vc, ows, wo, wot);
  hipFuncSetAttribute((const void*)oproj_kernel, hipFuncAttributeMaxDynamicSharedMemorySize, OP_LDS);
  oproj_kernel<<<192, 512, OP_LDS, stream>>>((const char*)ows, wot, out);
}
```

```cpp
#define MAPPING 0
#include <hip/hip_runtime.h>
#include <hip/hip_bf16.h>
#include <stdint.h>

typedef __attribute__((ext_vector_type(8))) short bf16x8;
typedef __attribute__((ext_vector_type(4))) short s16x4;
typedef __attribute__((ext_vector_type(16))) float f32x16;
typedef __attribute__((ext_vector_type(4))) float f32x4;
typedef __attribute__((ext_vector_type(2))) float f32x2;
typedef __attribute__((ext_vector_type(4))) unsigned u32x4;
typedef __attribute__((ext_vector_type(2))) unsigned u32x2;
typedef __attribute__((ext_vector_type(2))) __bf16 bf16x2_t;
typedef unsigned short u16;
typedef __attribute__((address_space(3))) s16x4* lds_s16x4_p;

#define DI __device__ __forceinline__

constexpr int NB = 16, SQ = 64, DM = 576, HQ = 32, HKV = 16, SK = 1024, DH = 128;
constexpr int MROWS = NB * SQ;
constexpr int QW = HQ * DH;

DI unsigned cvtpk(float lo, float hi) {
  f32x2 v = {lo, hi};
  bf16x2_t b = __builtin_convertvector(v, bf16x2_t);
  return __builtin_bit_cast(unsigned, b);
}
DI s16x4 tr_read(const char* p) {
  return __builtin_amdgcn_ds_read_tr16_b64_v4i16((lds_s16x4_p)(p));
}
DI f32x16 mfma32(bf16x8 a, bf16x8 b, f32x16 c) { return __builtin_amdgcn_mfma_f32_32x32x16_bf16(a, b, c, 0, 0, 0); }

constexpr int NS = 4;
constexpr int SLOT = 32768;
constexpr int ATT_LDS = 163840;
constexpr float SC = 0.08838834764831845f * 1.4426950408889634f;

DI void dma16(unsigned ldsaddr, unsigned voff, const void* sbase) {
  unsigned keep;
  asm volatile("s_mov_b32 %0, m0\n\ts_mov_b32 m0, %1\n\ts_nop 0\n\tglobal_load_lds_dwordx4 %2, %3 nt\n\ts_mov_b32 m0, %0"
               : "=&s"(keep) : "s"(ldsaddr), "v"(voff), "s"(sbase) : "memory");
}

DI void dma16p(unsigned ldsaddr, unsigned voff, const void* sbase) {
  unsigned keep;
  asm volatile("s_mov_b32 %0, m0\n\ts_mov_b32 m0, %1\n\ts_nop 0\n\tglobal_load_lds_dwordx4 %2, %3\n\ts_mov_b32 m0, %0"
               : "=&s"(keep) : "s"(ldsaddr), "v"(voff), "s"(sbase) : "memory");
}

struct AttnState {
  f32x16 o[4];
  f32x16 negm;
  float m, l;
};

template <int N> DI void wait_vm() { asm volatile("s_waitcnt vmcnt(%0)" ::"i"(N) : "memory"); }

struct AttnCtx {
  const char* kbase;
  const char* vbase;
  unsigned koff[2], voff[2];
  unsigned ldsw;
  unsigned kaddr[8];
  unsigned vaddr[8];
};

#define SBAR() __builtin_amdgcn_sched_barrier(0)
constexpr float THR = 8.0f;
template <int SL> DI void issue_k(const AttnCtx& c, int t) {
  const char* kb = c.kbase + (size_t)t * 16384;
  unsigned l0 = SL * 16384 + c.ldsw;
  dma16(l0, c.koff[0], kb);
  dma16(l0 + 1024, c.koff[1], kb);
}
template <int SL> DI void issue_v(const AttnCtx& c, int t) {
  const char* vb = c.vbase + (size_t)t * 16384;
  unsigned l0 = 65536 + SL * 16384 + c.ldsw;
  dma16(l0, c.voff[0], vb);
  dma16(l0 + 1024, c.voff[1], vb);
}
DI float rowmax16(const f32x16& s) {
  float pm = s[0];
#pragma unroll
  for (int i = 1; i < 16; ++i) pm = fmaxf(pm, s[i]);
  auto rr = __builtin_amdgcn_permlane32_swap(__float_as_uint(pm), __float_as_uint(pm), false, false);
  return fmaxf(__uint_as_float(rr[0]), __uint_as_float(rr[1]));
}
template <int SL, int WAITN, bool ISSUEK, bool ISSUEV, bool NEXT> DI void step(const AttnCtx& c, AttnState& st, f32x16& sc, const bf16x8* qf, const char* lds, int t) {
  wait_vm<WAITN>();
  asm volatile("s_waitcnt lgkmcnt(0)" ::: "memory");
  __builtin_amdgcn_s_barrier();
  SBAR();
  const char* Ks = lds + ((SL + 1) & 3) * 16384;
  const char* Vs = lds + SL * 16384;
  bf16x8 kf[8];
  if (NEXT) {
#pragma unroll
    for (int i = 0; i < 8; ++i) kf[i] = *(const bf16x8*)(Ks + c.kaddr[i]);
  }
  SBAR();
  if (ISSUEK) issue_k<SL>(c, t + 4);
  if (ISSUEV) issue_v<(SL + 3) & 3>(c, t + 3);
  SBAR();
  f32x16 sn;
  if (NEXT) {
    asm("v_mfma_f32_32x32x16_bf16 %0, %1, %2, %3" : "=&v"(sn) : "v"(kf[0]), "v"(qf[0]), "v"(st.negm));
#pragma unroll
    for (int i = 1; i < 8; ++i) sn = mfma32(kf[i], qf[i], sn);
  }
  s16x4 vlo[4][2], vhi[4][2];
#pragma unroll
  for (int k = 0; k < 2; ++k)
#pragma unroll
    for (int d = 0; d < 4; ++d) {
      vlo[d][k] = tr_read(Vs + c.vaddr[2 * d] + k * 4096);
      vhi[d][k] = tr_read(Vs + c.vaddr[2 * d + 1] + k * 4096);
    }
  float ps = 0.f;
#pragma unroll
  for (int i = 0; i < 16; ++i) { sc[i] = __builtin_amdgcn_exp2f(sc[i]); ps += sc[i]; }
  st.l += ps;
  bf16x8 pf[2];
#pragma unroll
  for (int k = 0; k < 2; ++k) {
    u32x4 w = {cvtpk(sc[8 * k], sc[8 * k + 1]), cvtpk(sc[8 * k + 2], sc[8 * k + 3]), cvtpk(sc[8 * k + 4], sc[8 * k + 5]), cvtpk(sc[8 * k + 6], sc[8 * k + 7])};
    pf[k] = __builtin_bit_cast(bf16x8, w);
  }
  if (NEXT) {
#pragma unroll
    for (int i = 0; i < 8; ++i) {
      __builtin_amdgcn_sched_group_barrier(0x8, 1, 0);
      __builtin_amdgcn_sched_group_barrier(0x100, 2, 0);
      __builtin_amdgcn_sched_group_barrier(0x2, 5, 0);
    }
  }
  SBAR();
#pragma unroll
  for (int k = 0; k < 2; ++k) {
#pragma unroll
    for (int d = 0; d < 4; ++d) {
      bf16x8 vf = __builtin_shufflevector(vlo[d][k], vhi[d][k], 0, 1, 2, 3, 4, 5, 6, 7);
      st.o[d] = mfma32(vf, pf[k], st.o[d]);
    }
  }
  if (NEXT) {
    const float pm = rowmax16(sn);
    SBAR();
    if (__builtin_expect(__any(pm > THR), 0)) {
      const float dl = fmaxf(pm, 0.f);
      const float alpha = __builtin_amdgcn_exp2f(-dl);
      st.m += dl;
      st.l *= alpha;
#pragma unroll
      for (int i = 0; i < 16; ++i) { sn[i] -= dl; st.negm[i] = -st.m; }
#pragma unroll
      for (int d = 0; d < 4; ++d)
#pragma unroll
        for (int i = 0; i < 16; ++i) st.o[d][i] *= alpha;
    }
    sc = sn;
  }
  SBAR();
}

__global__ __launch_bounds__(512, 2) void attn_kernel(const u16* __restrict__ x, const u16* __restrict__ wq, const u16* __restrict__ kc,
                                                      const u16* __restrict__ vc, u16* __restrict__ og,
                                                      const u16* __restrict__ wo, char* __restrict__ wot) {
  extern __shared__ __attribute__((aligned(16))) char lds[];
  const int bid = blockIdx.x;
  const int hkv = 2 * (bid & 7) + ((bid >> 3) & 1), b = bid >> 4;
  const int rot = 2 * ((b * 9) >> 4);
  const int tid = threadIdx.x, lane = tid & 63, r = lane & 31, h = lane >> 5;
  const int wid = __builtin_amdgcn_readfirstlane(tid >> 6);
  const int kg = wid >> 2, g = (wid >> 1) & 1, pb = wid & 1;
  const int head = hkv * 2 + g;

  AttnCtx c;
  c.kbase = (const char*)(kc + (size_t)(b * HKV + hkv) * SK * DH);
  c.vbase = (const char*)(vc + (size_t)(b * HKV + hkv) * SK * DH);
  c.ldsw = wid * 8 * 256;
#pragma unroll
  for (int i = 0; i < 2; ++i) {
    const int row = 8 * wid + 4 * i + (lane >> 4), pos = lane & 15;
    c.koff[i] = row * 256 + ((pos ^ (row & 15)) << 4);
    c.voff[i] = row * 256 + ((pos ^ (((row & 3) << 2) | ((row >> 2) & 3))) << 4);
  }
  {
    const int krow = 32 * kg + r;
#pragma unroll
    for (int i = 0; i < 8; ++i) c.kaddr[i] = krow * 256 + (((2 * i + h) ^ (r & 15)) << 4);
    const int blk = (lane >> 4) & 1, q4 = (lane & 15) >> 2, p4 = lane & 3;
#pragma unroll
    for (int d = 0; d < 4; ++d)
#pragma unroll
      for (int t = 0; t < 2; ++t) {
        const int row = 32 * kg + 8 * t + 4 * h + q4;
        const int ch = 4 * d + 2 * blk + (p4 >> 1);
        const int sw = ((row & 3) << 2) | ((row >> 2) & 3);
        c.vaddr[2 * d + t] = 65536 + row * 256 + ((ch ^ sw) << 4) + 8 * (p4 & 1);
        asm volatile("" : "+v"(c.vaddr[2 * d + t]));
      }
  }
  const char* wqb = (const char*)(wq + (size_t)hkv * 256);
  const char* xb = (const char*)(x + (size_t)b * SQ * DM);
  constexpr unsigned XB0 = 131072;
  unsigned wsrc[2], xsrc;
#pragma unroll
  for (int i = 0; i < 2; ++i) {
    const int row = 4 * wid + 2 * i + (lane >> 5), pos = lane & 31;
    const int f = ((row & 3) << 2) | ((row >> 2) & 3);
    wsrc[i] = row * 8192 + ((pos ^ f) << 4);
  }
  {
    const int row = 8 * wid + (lane >> 3), pos = lane & 7;
    xsrc = row * 1152 + ((pos ^ ((row >> 1) & 7)) << 4);
  }
  auto issue_w = [&](int chi, unsigned wbuf) {
    int ch = chi + rot; if (ch >= 18) ch -= 18;
    const char* wsb = wqb + (size_t)ch * 32 * 8192;
#pragma unroll
    for (int i = 0; i < 2; ++i) dma16p(wbuf + wid * 2048 + i * 1024, wsrc[i], wsb);
  };
  auto issue_x = [&](int p) {
    int ch = 2 * p + rot; if (ch >= 18) ch -= 18;
    dma16p(XB0 + (p & 3) * 8192 + wid * 1024, xsrc, xb + ch * 64);
  };
  constexpr unsigned PS[8] = {6, 7, 0, 1, 4, 2, 5, 3};
#pragma unroll
  for (int p = 0; p < 4; ++p) { issue_w(2 * p, PS[2 * p] * 16384); issue_w(2 * p + 1, PS[2 * p + 1] * 16384); issue_x(p); }
  unsigned waddr[2][2], xaddr[4];
  {
    const int blk = (lane >> 4) & 1, q4 = (lane & 15) >> 2, p4 = lane & 3;
#pragma unroll
    for (int j = 0; j < 2; ++j)
#pragma unroll
      for (int t = 0; t < 2; ++t) {
        const int row = 8 * h + 4 * t + q4;
        const int chn = 4 * (4 * g + 2 * kg + j) + 2 * blk + (p4 >> 1);
        const int f = ((row & 3) << 2) | ((row >> 2) & 3);
        waddr[j][t] = row * 512 + ((chn ^ f) << 4) + 8 * (p4 & 1);
      }
    const int row = 32 * pb + r;
#pragma unroll
    for (int s = 0; s < 4; ++s) xaddr[s] = row * 128 + (((2 * s + h) ^ ((row >> 1) & 7)) << 4);
  }
  f32x16 qa[2] = {};
  auto proj_chunk = [&](unsigned wbuf, unsigned xbuf, int e) {
#pragma unroll
    for (int s = 0; s < 2; ++s) {
      const bf16x8 xf = *(const bf16x8*)(lds + xbuf + xaddr[2 * e + s]);
#pragma unroll
      for (int j = 0; j < 2; ++j) {
        s16x4 lo = tr_read(lds + wbuf + s * 8192 + waddr[j][0]);
        s16x4 hi = tr_read(lds + wbuf + s * 8192 + waddr[j][1]);
        qa[j] = mfma32(__builtin_shufflevector(lo, hi, 0, 1, 2, 3, 4, 5, 6, 7), xf, qa[j]);
      }
    }
  };
  constexpr int PW[18] = {15, 15, 12, 15, 12, 15, 12, 15, 14, 15, 14, 15, 14, 14, 13, 13, 12, 12};
#pragma unroll
  for (int ch = 0; ch < 18; ++ch) {
    {
      asm volatile("s_waitcnt vmcnt(%0)" ::"i"(PW[ch]) : "memory");
    }
    asm volatile("s_waitcnt lgkmcnt(0)" ::: "memory");
    __builtin_amdgcn_s_barrier();
    if (ch >= 2 && (ch & 1) == 0 && ch / 2 + 3 <= 8) issue_x(ch / 2 + 3);
    if (ch >= 1 && ch <= 10) issue_w(ch + 7, PS[(ch + 7) & 7] * 16384);
    if (ch == 11) issue_k<0>(c, 0);
    if (ch == 12) issue_k<1>(c, 1);
    if (ch == 13) issue_v<0>(c, 0);
    if (ch == 14) issue_k<2>(c, 2);
    if (ch == 15) issue_v<1>(c, 1);
    if (ch == 16) issue_k<3>(c, 3);
    if (ch == 17) issue_v<2>(c, 2);
    proj_chunk(PS[ch & 7] * 16384, XB0 + ((ch >> 1) & 3) * 8192, ch & 1);
  }
  asm volatile("s_waitcnt lgkmcnt(0)" ::: "memory");
  __builtin_amdgcn_s_barrier();
  {
    char* qt = lds + 131072 + (g * 2 + pb) * 8192 + r * 256;
#pragma unroll
    for (int j = 0; j < 2; ++j)
#pragma unroll
      for (int a = 0; a < 4; ++a) {
        unsigned w0 = cvtpk(qa[j][4 * a], qa[j][4 * a + 1]), w1 = cvtpk(qa[j][4 * a + 2], qa[j][4 * a + 3]);
        w0 = cvtpk(__uint_as_float(w0 << 16) * SC, __uint_as_float(w0 & 0xffff0000u) * SC);
        w1 = cvtpk(__uint_as_float(w1 << 16) * SC, __uint_as_float(w1 & 0xffff0000u) * SC);
        u32x2 w = {w0, w1};
        *(u32x2*)(qt + (((8 * kg + 4 * j + a) ^ (r & 15)) << 4) + 8 * h) = w;
      }
  }
  wait_vm<12>();
  asm volatile("s_waitcnt lgkmcnt(0)" ::: "memory");
  __builtin_amdgcn_s_barrier();
  bf16x8 qf[8];
  {
    const char* qt = lds + 131072 + (g * 2 + pb) * 8192 + r * 256;
#pragma unroll
    for (int i = 0; i < 8; ++i) qf[i] = *(const bf16x8*)(qt + (((2 * i + h) ^ (r & 15)) << 4));
  }

  if (wid >= 4) __builtin_amdgcn_s_setprio(1);
  AttnState st;
#pragma unroll
  for (int d = 0; d < 4; ++d) st.o[d] = f32x16{};
  st.m = -1e30f; st.l = 0.f;

  f32x16 sc = {};
#pragma unroll
  for (int i = 0; i < 8; ++i) sc = mfma32(*(const bf16x8*)(lds + c.kaddr[i]), qf[i], sc);
  st.m = rowmax16(sc);
#pragma unroll
  for (int i = 0; i < 16; ++i) { sc[i] -= st.m; st.negm[i] = -st.m; }
  step<0, 8, true, true, true>(c, st, sc, qf, lds, 0);
  step<1, 8, true, true, true>(c, st, sc, qf, lds, 1);
  {
    const char* wsl = (const char*)(wo + (size_t)(32 * (bid >> 1)) * DM + 288 * (bid & 1));
#pragma unroll
    for (int i = 0; i < 3; ++i) {
      const int p = wid + 8 * i;
      const int idx = p * 64 + lane, row = idx / 36, chn = idx - row * 36;
      dma16(131072 + p * 1024, p < 18 ? (unsigned)(row * 1152 + chn * 16) : 0u, wsl);
    }
  }
  step<2, 8, true, true, true>(c, st, sc, qf, lds, 2);
  step<3, 8, true, true, true>(c, st, sc, qf, lds, 3);
#pragma unroll 1
  for (int t = 4; t < 12; t += 4) {
    step<0, 8, true, true, true>(c, st, sc, qf, lds, t);
    step<1, 8, true, true, true>(c, st, sc, qf, lds, t + 1);
    step<2, 8, true, true, true>(c, st, sc, qf, lds, t + 2);
    step<3, 8, true, true, true>(c, st, sc, qf, lds, t + 3);
  }
  {
    const int wk = bid >> 1;
    const int g4 = lane >> 4, q4 = (lane & 15) >> 2, p4 = lane & 3;
    const char* sb = lds + 131072 + (8 * g4 + q4) * 576 + p4 * 8;
#pragma unroll
    for (int i = 0; i < 3; ++i) {
      const int cc = wid + 8 * i;
      if (cc < 18) {
        s16x4 lo = tr_read(sb + cc * 32), hi = tr_read(sb + cc * 32 + 4 * 576);
        bf16x8 af = __builtin_shufflevector(lo, hi, 0, 1, 2, 3, 4, 5, 6, 7);
        *(bf16x8*)(wot + ((size_t)(wk * 36 + (bid & 1) * 18 + cc) << 10) + lane * 16) = af;
      }
    }
  }
  step<0, 8, false, true, true>(c, st, sc, qf, lds, 12);
  step<1, 6, false, false, true>(c, st, sc, qf, lds, 13);
  step<2, 2, false, false, true>(c, st, sc, qf, lds, 14);
  step<3, 0, false, false, false>(c, st, sc, qf, lds, 15);

  float lt = st.l + __shfl_xor(st.l, 32);
  __syncthreads();
  char* xw = lds + wid * 8704;
  char* xp = lds + (wid ^ 4) * 8704;
  {
#pragma unroll
    for (int dd = 0; dd < 2; ++dd) {
      const f32x16 v = kg ? st.o[dd] : st.o[2 + dd];
#pragma unroll
      for (int a = 0; a < 4; ++a) {
        f32x4 w = {v[4 * a], v[4 * a + 1], v[4 * a + 2], v[4 * a + 3]};
        *(f32x4*)(xw + ((dd * 4 + a) * 64 + lane) * 16) = w;
      }
    }
    f32x2 ml = {st.m, lt};
    *(f32x2*)(xw + 8192 + lane * 8) = ml;
  }
  __syncthreads();
  f32x2 pml = *(const f32x2*)(xp + 8192 + lane * 8);
  const float mm = fmaxf(st.m, pml[0]);
  const float fa = __builtin_amdgcn_exp2f(st.m - mm), fb = __builtin_amdgcn_exp2f(pml[0] - mm);
  const float inv = 1.0f / (lt * fa + pml[1] * fb);
  const float wa = fa * inv, wb = fb * inv;
  char* stg = lds + 8 * 8704 + wid * 4608;
#pragma unroll
  for (int dd = 0; dd < 2; ++dd) {
    const f32x16 own = kg ? st.o[2 + dd] : st.o[dd];
#pragma unroll
    for (int a = 0; a < 4; ++a) {
      f32x4 pv = *(const f32x4*)(xp + ((dd * 4 + a) * 64 + lane) * 16);
      float o0 = own[4 * a] * wa + pv[0] * wb, o1 = own[4 * a + 1] * wa + pv[1] * wb;
      float o2 = own[4 * a + 2] * wa + pv[2] * wb, o3 = own[4 * a + 3] * wa + pv[3] * wb;
      u32x2 w = {cvtpk(o0, o1), cvtpk(o2, o3)};
      *(u32x2*)(stg + r * 144 + (32 * dd + 8 * a + 4 * h) * 2) = w;
    }
  }
  asm volatile("s_waitcnt lgkmcnt(0)" ::: "memory");
  __builtin_amdgcn_wave_barrier();
  {
    const int kst = (head * DH + kg * 64) >> 5;
    const int mt0 = (b * SQ + 32 * pb) >> 4;
#pragma unroll
    for (int i = 0; i < 4; ++i) {
      const int mi = i >> 1, ki = i & 1;
      u32x4 w = *(const u32x4*)(stg + (16 * mi + (lane & 15)) * 144 + ki * 64 + (lane >> 4) * 16);
      char* dst = (char*)og + ((size_t)((kst + ki) * 64 + mt0 + mi) << 10) + lane * 16;
      *(u32x4*)dst = w;
    }
  }
}

DI f32x4 mfma16(bf16x8 a, bf16x8 b, f32x4 c) { return __builtin_amdgcn_mfma_f32_16x16x32_bf16(a, b, c, 0, 0, 0); }
constexpr int OP_LDS = 98304;
__global__ __launch_bounds__(512, 2) void oproj_kernel(const char* __restrict__ og, const char* __restrict__ wot, u16* __restrict__ out) {
  extern __shared__ __attribute__((aligned(16))) char lds[];
  const int bid = blockIdx.x;
  const int xc = bid & 7, j = bid >> 3;
  const int mt = (xc & 3) * 4 + (j & 3), nt = (xc >> 2) * 6 + (j >> 2);
  const int tid = threadIdx.x, lane = tid & 63;
  const int wid = __builtin_amdgcn_readfirstlane(tid >> 6);
  const int rot = ((j >> 2) + 3 * (j & 3)) & 7;
  const unsigned voff = lane * 16;
  const char* ob = og + ((size_t)((8 * wid) * 64 + 4 * mt) << 10);
  const char* wb = wot + ((size_t)((8 * wid) * 36 + 3 * nt) << 10);
  bf16x8 fb[4][4], fa[4][3];
  auto issue = [&](int s) {
    const int ks = (s >> 3) * 64 + ((s + rot) & 7);
    const char* po = ob + (size_t)ks * 65536;
    const char* pw = wb + (size_t)ks * 36864;
#pragma unroll
    for (int i = 0; i < 4; ++i) asm volatile("global_load_dwordx4 %0, %1, %2 offset:%3" : "=v"(fb[s & 3][i]) : "v"(voff), "s"(po), "i"(i * 1024) : "memory");
#pragma unroll
    for (int i = 0; i < 3; ++i) asm volatile("global_load_dwordx4 %0, %1, %2 offset:%3" : "=v"(fa[s & 3][i]) : "v"(voff), "s"(pw), "i"(i * 1024) : "memory");
  };
#pragma unroll
  for (int s = 0; s < 4; ++s) issue(s);
  f32x4 acc[4][3];
#pragma unroll
  for (int i = 0; i < 4; ++i)
#pragma unroll
    for (int c = 0; c < 3; ++c) acc[i][c] = f32x4{0.f, 0.f, 0.f, 0.f};
#pragma unroll
  for (int s = 0; s < 16; ++s) {
    const int young = 7 * ((15 - s) < 3 ? (15 - s) : 3);
    asm volatile("s_waitcnt vmcnt(%7)" : "+v"(fb[s & 3][0]), "+v"(fb[s & 3][1]), "+v"(fb[s & 3][2]), "+v"(fb[s & 3][3]), "+v"(fa[s & 3][0]), "+v"(fa[s & 3][1]), "+v"(fa[s & 3][2]) : "i"(young) : "memory");
#pragma unroll
    for (int i = 0; i < 4; ++i)
#pragma unroll
      for (int c = 0; c < 3; ++c) acc[i][c] = mfma16(fa[s & 3][c], fb[s & 3][i], acc[i][c]);
    __builtin_amdgcn_sched_barrier(0);
    if (s + 4 < 16) issue(s + 4);
    __builtin_amdgcn_sched_barrier(0);
  }
  {
    char* xs = lds + wid * 12288 + lane * 16;
#pragma unroll
    for (int i = 0; i < 4; ++i)
#pragma unroll
      for (int c = 0; c < 3; ++c) *(f32x4*)(xs + (i * 3 + c) * 1024) = acc[i][c];
  }
  __syncthreads();
#pragma unroll
  for (int it = 0; it < 2; ++it) {
    const int ci = tid + 512 * it;
    if (ci < 768) {
      f32x4 sum = *(const f32x4*)(lds + ci * 16);
#pragma unroll
      for (int w = 1; w < 8; ++w) sum += *(const f32x4*)(lds + w * 12288 + ci * 16);
      const int tl = ci >> 6, ln = ci & 63, mi = tl / 3, c = tl - mi * 3;
      u32x2 w2 = {cvtpk(sum[0], sum[1]), cvtpk(sum[2], sum[3])};
      *(u32x2*)(out + (size_t)(64 * mt + 16 * mi + (ln & 15)) * DM + 48 * nt + 16 * c + 4 * (ln >> 4)) = w2;
    }
  }
}

extern "C" void kernel_launch(void* const* d_in, const int* in_sizes, int n_in, void* d_out, int out_size, void* d_ws, size_t ws_size,
                              hipStream_t stream) {
  const u16* x = (const u16*)d_in[0];
  const u16* kc = (const u16*)d_in[1];
  const u16* vc = (const u16*)d_in[2];
  const u16* wq = (const u16*)d_in[3];
  const u16* wo = (const u16*)d_in[4];
  u16* out = (u16*)d_out;
  u16* qws = (u16*)d_ws;
  u16* ows = qws + (size_t)MROWS * QW;
  hipFuncSetAttribute((const void*)attn_kernel, hipFuncAttributeMaxDynamicSharedMemorySize, ATT_LDS);
  char* wot = (char*)d_ws + (24 << 20);
  attn_kernel<<<256, 512, ATT_LDS, stream>>>(x, wq, kc, vc, ows, wo, wot);
  hipFuncSetAttribute((const void*)oproj_kernel, hipFuncAttributeMaxDynamicSharedMemorySize, OP_LDS);
  oproj_kernel<<<192, 512, OP_LDS, stream>>>((const char*)ows, wot, out);
}
```

```cpp
#define MAPPING 1
#include <hip/hip_runtime.h>
#include <hip/hip_bf16.h>
#include <stdint.h>

typedef __attribute__((ext_vector_type(8))) short bf16x8;
typedef __attribute__((ext_vector_type(4))) short s16x4;
typedef __attribute__((ext_vector_type(16))) float f32x16;
typedef __attribute__((ext_vector_type(4))) float f32x4;
typedef __attribute__((ext_vector_type(2))) float f32x2;
typedef __attribute__((ext_vector_type(4))) unsigned u32x4;
typedef __attribute__((ext_vector_type(2))) unsigned u32x2;
typedef __attribute__((ext_vector_type(2))) __bf16 bf16x2_t;
typedef unsigned short u16;
typedef __attribute__((address_space(3))) s16x4* lds_s16x4_p;

#define DI __device__ __forceinline__

constexpr int NB = 16, SQ = 64, DM = 576, HQ = 32, HKV = 16, SK = 1024, DH = 128;
constexpr int MROWS = NB * SQ;
constexpr int QW = HQ * DH;

DI unsigned cvtpk(float lo, float hi) {
  f32x2 v = {lo, hi};
  bf16x2_t b = __builtin_convertvector(v, bf16x2_t);
  return __builtin_bit_cast(unsigned, b);
}
DI s16x4 tr_read(const char* p) {
  return __builtin_amdgcn_ds_read_tr16_b64_v4i16((lds_s16x4_p)(p));
}
DI f32x16 mfma32(bf16x8 a, bf16x8 b, f32x16 c) { return __builtin_amdgcn_mfma_f32_32x32x16_bf16(a, b, c, 0, 0, 0); }

constexpr int NS = 4;
constexpr int SLOT = 32768;
constexpr int ATT_LDS = 163840;
constexpr float SC = 0.08838834764831845f * 1.4426950408889634f;

DI void dma16(unsigned ldsaddr, unsigned voff, const void* sbase) {
  unsigned keep;
  asm volatile("s_mov_b32 %0, m0\n\ts_mov_b32 m0, %1\n\ts_nop 0\n\tglobal_load_lds_dwordx4 %2, %3 nt\n\ts_mov_b32 m0, %0"
               : "=&s"(keep) : "s"(ldsaddr), "v"(voff), "s"(sbase) : "memory");
}

DI void dma16p(unsigned ldsaddr, unsigned voff, const void* sbase) {
  unsigned keep;
  asm volatile("s_mov_b32 %0, m0\n\ts_mov_b32 m0, %1\n\ts_nop 0\n\tglobal_load_lds_dwordx4 %2, %3\n\ts_mov_b32 m0, %0"
               : "=&s"(keep) : "s"(ldsaddr), "v"(voff), "s"(sbase) : "memory");
}

struct AttnState {
  f32x16 o[4];
  f32x16 negm;
  float m, l;
};

template <int N> DI void wait_vm() { asm volatile("s_waitcnt vmcnt(%0)" ::"i"(N) : "memory"); }

struct AttnCtx {
  const char* kbase;
  const char* vbase;
  unsigned koff[2], voff[2];
  unsigned ldsw;
  unsigned kaddr[8];
  unsigned vaddr[8];
};

#define SBAR() __builtin_amdgcn_sched_barrier(0)
constexpr float THR = 8.0f;
template <int SL> DI void issue_k(const AttnCtx& c, int t) {
  const char* kb = c.kbase + (size_t)t * 16384;
  unsigned l0 = SL * 16384 + c.ldsw;
  dma16(l0, c.koff[0], kb);
  dma16(l0 + 1024, c.koff[1], kb);
}
template <int SL> DI void issue_v(const AttnCtx& c, int t) {
  const char* vb = c.vbase + (size_t)t * 16384;
  unsigned l0 = 65536 + SL * 16384 + c.ldsw;
  dma16(l0, c.voff[0], vb);
  dma16(l0 + 1024, c.voff[1], vb);
}
DI float rowmax16(const f32x16& s) {
  float pm = s[0];
#pragma unroll
  for (int i = 1; i < 16; ++i) pm = fmaxf(pm, s[i]);
  auto rr = __builtin_amdgcn_permlane32_swap(__float_as_uint(pm), __float_as_uint(pm), false, false);
  return fmaxf(__uint_as_float(rr[0]), __uint_as_float(rr[1]));
}
template <int SL, int WAITN, bool ISSUEK, bool ISSUEV, bool NEXT> DI void step(const AttnCtx& c, AttnState& st, f32x16& sc, const bf16x8* qf, const char* lds, int t) {
  wait_vm<WAITN>();
  asm volatile("s_waitcnt lgkmcnt(0)" ::: "memory");
  __builtin_amdgcn_s_barrier();
  SBAR();
  const char* Ks = lds + ((SL + 1) & 3) * 16384;
  const char* Vs = lds + SL * 16384;
  bf16x8 kf[8];
  if (NEXT) {
#pragma unroll
    for (int i = 0; i < 8; ++i) kf[i] = *(const bf16x8*)(Ks + c.kaddr[i]);
  }
  SBAR();
  if (ISSUEK) issue_k<SL>(c, t + 4);
  if (ISSUEV) issue_v<(SL + 3) & 3>(c, t + 3);
  SBAR();
  f32x16 sn;
  if (NEXT) {
    asm("v_mfma_f32_32x32x16_bf16 %0, %1, %2, %3" : "=&v"(sn) : "v"(kf[0]), "v"(qf[0]), "v"(st.negm));
#pragma unroll
    for (int i = 1; i < 8; ++i) sn = mfma32(kf[i], qf[i], sn);
  }
  s16x4 vlo[4][2], vhi[4][2];
#pragma unroll
  for (int k = 0; k < 2; ++k)
#pragma unroll
    for (int d = 0; d < 4; ++d) {
      vlo[d][k] = tr_read(Vs + c.vaddr[2 * d] + k * 4096);
      vhi[d][k] = tr_read(Vs + c.vaddr[2 * d + 1] + k * 4096);
    }
  float ps = 0.f;
#pragma unroll
  for (int i = 0; i < 16; ++i) { sc[i] = __builtin_amdgcn_exp2f(sc[i]); ps += sc[i]; }
  st.l += ps;
  bf16x8 pf[2];
#pragma unroll
  for (int k = 0; k < 2; ++k) {
    u32x4 w = {cvtpk(sc[8 * k], sc[8 * k + 1]), cvtpk(sc[8 * k + 2], sc[8 * k + 3]), cvtpk(sc[8 * k + 4], sc[8 * k + 5]), cvtpk(sc[8 * k + 6], sc[8 * k + 7])};
    pf[k] = __builtin_bit_cast(bf16x8, w);
  }
  if (NEXT) {
#pragma unroll
    for (int i = 0; i < 8; ++i) {
      __builtin_amdgcn_sched_group_barrier(0x8, 1, 0);
      __builtin_amdgcn_sched_group_barrier(0x100, 2, 0);
      __builtin_amdgcn_sched_group_barrier(0x2, 5, 0);
    }
  }
  SBAR();
#pragma unroll
  for (int k = 0; k < 2; ++k) {
#pragma unroll
    for (int d = 0; d < 4; ++d) {
      bf16x8 vf = __builtin_shufflevector(vlo[d][k], vhi[d][k], 0, 1, 2, 3, 4, 5, 6, 7);
      st.o[d] = mfma32(vf, pf[k], st.o[d]);
    }
  }
  if (NEXT) {
    const float pm = rowmax16(sn);
    SBAR();
    if (__builtin_expect(__any(pm > THR), 0)) {
      const float dl = fmaxf(pm, 0.f);
      const float alpha = __builtin_amdgcn_exp2f(-dl);
      st.m += dl;
      st.l *= alpha;
#pragma unroll
      for (int i = 0; i < 16; ++i) { sn[i] -= dl; st.negm[i] = -st.m; }
#pragma unroll
      for (int d = 0; d < 4; ++d)
#pragma unroll
        for (int i = 0; i < 16; ++i) st.o[d][i] *= alpha;
    }
    sc = sn;
  }
  SBAR();
}

__global__ __launch_bounds__(512, 2) void attn_kernel(const u16* __restrict__ x, const u16* __restrict__ wq, const u16* __restrict__ kc,
                                                      const u16* __restrict__ vc, u16* __restrict__ og,
                                                      const u16* __restrict__ wo, char* __restrict__ wot) {
  extern __shared__ __attribute__((aligned(16))) char lds[];
  const int bid = blockIdx.x;
  const int hkv = (bid & 1) * 8 + ((bid >> 3) & 7), b = ((bid >> 1) & 3) * 4 + (bid >> 6);
  const int rot = 2 * (((b & 3) * 9) >> 2);
  const int tid = threadIdx.x, lane = tid & 63, r = lane & 31, h = lane >> 5;
  const int wid = __builtin_amdgcn_readfirstlane(tid >> 6);
  const int kg = wid >> 2, g = (wid >> 1) & 1, pb = wid & 1;
  const int head = hkv * 2 + g;

  AttnCtx c;
  c.kbase = (const char*)(kc + (size_t)(b * HKV + hkv) * SK * DH);
  c.vbase = (const char*)(vc + (size_t)(b * HKV + hkv) * SK * DH);
  c.ldsw = wid * 8 * 256;
#pragma unroll
  for (int i = 0; i < 2; ++i) {
    const int row = 8 * wid + 4 * i + (lane >> 4), pos = lane & 15;
    c.koff[i] = row * 256 + ((pos ^ (row & 15)) << 4);
    c.voff[i] = row * 256 + ((pos ^ (((row & 3) << 2) | ((row >> 2) & 3))) << 4);
  }
  {
    const int krow = 32 * kg + r;
#pragma unroll
    for (int i = 0; i < 8; ++i) c.kaddr[i] = krow * 256 + (((2 * i + h) ^ (r & 15)) << 4);
    const int blk = (lane >> 4) & 1, q4 = (lane & 15) >> 2, p4 = lane & 3;
#pragma unroll
    for (int d = 0; d < 4; ++d)
#pragma unroll
      for (int t = 0; t < 2; ++t) {
        const int row = 32 * kg + 8 * t + 4 * h + q4;
        const int ch = 4 * d + 2 * blk + (p4 >> 1);
        const int sw = ((row & 3) << 2) | ((row >> 2) & 3);
        c.vaddr[2 * d + t] = 65536 + row * 256 + ((ch ^ sw) << 4) + 8 * (p4 & 1);
        asm volatile("" : "+v"(c.vaddr[2 * d + t]));
      }
  }
  const char* wqb = (const char*)(wq + (size_t)hkv * 256);
  const char* xb = (const char*)(x + (size_t)b * SQ * DM);
  constexpr unsigned XB0 = 131072;
  unsigned wsrc[2], xsrc;
#pragma unroll
  for (int i = 0; i < 2; ++i) {
    const int row = 4 * wid + 2 * i + (lane >> 5), pos = lane & 31;
    const int f = ((row & 3) << 2) | ((row >> 2) & 3);
    wsrc[i] = row * 8192 + ((pos ^ f) << 4);
  }
  {
    const int row = 8 * wid + (lane >> 3), pos = lane & 7;
    xsrc = row * 1152 + ((pos ^ ((row >> 1) & 7)) << 4);
  }
  auto issue_w = [&](int chi, unsigned wbuf) {
    int ch = chi + rot; if (ch >= 18) ch -= 18;
    const char* wsb = wqb + (size_t)ch * 32 * 8192;
#pragma unroll
    for (int i = 0; i < 2; ++i) dma16p(wbuf + wid * 2048 + i * 1024, wsrc[i], wsb);
  };
  auto issue_x = [&](int p) {
    int ch = 2 * p + rot; if (ch >= 18) ch -= 18;
    dma16p(XB0 + (p & 3) * 8192 + wid * 1024, xsrc, xb + ch * 64);
  };
  constexpr unsigned PS[8] = {6, 7, 0, 1, 4, 2, 5, 3};
#pragma unroll
  for (int p = 0; p < 4; ++p) { issue_w(2 * p, PS[2 * p] * 16384); issue_w(2 * p + 1, PS[2 * p + 1] * 16384); issue_x(p); }
  unsigned waddr[2][2], xaddr[4];
  {
    const int blk = (lane >> 4) & 1, q4 = (lane & 15) >> 2, p4 = lane & 3;
#pragma unroll
    for (int j = 0; j < 2; ++j)
#pragma unroll
      for (int t = 0; t < 2; ++t) {
        const int row = 8 * h + 4 * t + q4;
        const int chn = 4 * (4 * g + 2 * kg + j) + 2 * blk + (p4 >> 1);
        const int f = ((row & 3) << 2) | ((row >> 2) & 3);
        waddr[j][t] = row * 512 + ((chn ^ f) << 4) + 8 * (p4 & 1);
      }
    const int row = 32 * pb + r;
#pragma unroll
    for (int s = 0; s < 4; ++s) xaddr[s] = row * 128 + (((2 * s + h) ^ ((row >> 1) & 7)) << 4);
  }
  f32x16 qa[2] = {};
  auto proj_chunk = [&](unsigned wbuf, unsigned xbuf, int e) {
#pragma unroll
    for (int s = 0; s < 2; ++s) {
      const bf16x8 xf = *(const bf16x8*)(lds + xbuf + xaddr[2 * e + s]);
#pragma unroll
      for (int j = 0; j < 2; ++j) {
        s16x4 lo = tr_read(lds + wbuf + s * 8192 + waddr[j][0]);
        s16x4 hi = tr_read(lds + wbuf + s * 8192 + waddr[j][1]);
        qa[j] = mfma32(__builtin_shufflevector(lo, hi, 0, 1, 2, 3, 4, 5, 6, 7), xf, qa[j]);
      }
    }
  };
  constexpr int PW[18] = {15, 15, 12, 15, 12, 15, 12, 15, 14, 15, 14, 15, 14, 14, 13, 13, 12, 12};
#pragma unroll
  for (int ch = 0; ch < 18; ++ch) {
    {
      asm volatile("s_waitcnt vmcnt(%0)" ::"i"(PW[ch]) : "memory");
    }
    asm volatile("s_waitcnt lgkmcnt(0)" ::: "memory");
    __builtin_amdgcn_s_barrier();
    if (ch >= 2 && (ch & 1) == 0 && ch / 2 + 3 <= 8) issue_x(ch / 2 + 3);
    if (ch >= 1 && ch <= 10) issue_w(ch + 7, PS[(ch + 7) & 7] * 16384);
    if (ch == 11) issue_k<0>(c, 0);
    if (ch == 12) issue_k<1>(c, 1);
    if (ch == 13) issue_v<0>(c, 0);
    if (ch == 14) issue_k<2>(c, 2);
    if (ch == 15) issue_v<1>(c, 1);
    if (ch == 16) issue_k<3>(c, 3);
    if (ch == 17) issue_v<2>(c, 2);
    proj_chunk(PS[ch & 7] * 16384, XB0 + ((ch >> 1) & 3) * 8192, ch & 1);
  }
  asm volatile("s_waitcnt lgkmcnt(0)" ::: "memory");
  __builtin_amdgcn_s_barrier();
  {
    char* qt = lds + 131072 + (g * 2 + pb) * 8192 + r * 256;
#pragma unroll
    for (int j = 0; j < 2; ++j)
#pragma unroll
      for (int a = 0; a < 4; ++a) {
        unsigned w0 = cvtpk(qa[j][4 * a], qa[j][4 * a + 1]), w1 = cvtpk(qa[j][4 * a + 2], qa[j][4 * a + 3]);
        w0 = cvtpk(__uint_as_float(w0 << 16) * SC, __uint_as_float(w0 & 0xffff0000u) * SC);
        w1 = cvtpk(__uint_as_float(w1 << 16) * SC, __uint_as_float(w1 & 0xffff0000u) * SC);
        u32x2 w = {w0, w1};
        *(u32x2*)(qt + (((8 * kg + 4 * j + a) ^ (r & 15)) << 4) + 8 * h) = w;
      }
  }
  wait_vm<12>();
  asm volatile("s_waitcnt lgkmcnt(0)" ::: "memory");
  __builtin_amdgcn_s_barrier();
  bf16x8 qf[8];
  {
    const char* qt = lds + 131072 + (g * 2 + pb) * 8192 + r * 256;
#pragma unroll
    for (int i = 0; i < 8; ++i) qf[i] = *(const bf16x8*)(qt + (((2 * i + h) ^ (r & 15)) << 4));
  }

  AttnState st;
#pragma unroll
  for (int d = 0; d < 4; ++d) st.o[d] = f32x16{};
  st.m = -1e30f; st.l = 0.f;

  f32x16 sc = {};
#pragma unroll
  for (int i = 0; i < 8; ++i) sc = mfma32(*(const bf16x8*)(lds + c.kaddr[i]), qf[i], sc);
  st.m = rowmax16(sc);
#pragma unroll
  for (int i = 0; i < 16; ++i) { sc[i] -= st.m; st.negm[i] = -st.m; }
  step<0, 8, true, true, true>(c, st, sc, qf, lds, 0);
  step<1, 8, true, true, true>(c, st, sc, qf, lds, 1);
  {
    const char* wsl = (const char*)(wo + (size_t)(32 * (bid >> 1)) * DM + 288 * (bid & 1));
#pragma unroll
    for (int i = 0; i < 3; ++i) {
      const int p = wid + 8 * i;
      const int idx = p * 64 + lane, row = idx / 36, chn = idx - row * 36;
      dma16(131072 + p * 1024, p < 18 ? (unsigned)(row * 1152 + chn * 16) : 0u, wsl);
    }
  }
  step<2, 8, true, true, true>(c, st, sc, qf, lds, 2);
  step<3, 8, true, true, true>(c, st, sc, qf, lds, 3);
#pragma unroll 1
  for (int t = 4; t < 12; t += 4) {
    step<0, 8, true, true, true>(c, st, sc, qf, lds, t);
    step<1, 8, true, true, true>(c, st, sc, qf, lds, t + 1);
    step<2, 8, true, true, true>(c, st, sc, qf, lds, t + 2);
    step<3, 8, true, true, true>(c, st, sc, qf, lds, t + 3);
  }
  {
    const int wk = bid >> 1;
    const int g4 = lane >> 4, q4 = (lane & 15) >> 2, p4 = lane & 3;
    const char* sb = lds + 131072 + (8 * g4 + q4) * 576 + p4 * 8;
#pragma unroll
    for (int i = 0; i < 3; ++i) {
      const int cc = wid + 8 * i;
      if (cc < 18) {
        s16x4 lo = tr_read(sb + cc * 32), hi = tr_read(sb + cc * 32 + 4 * 576);
        bf16x8 af = __builtin_shufflevector(lo, hi, 0, 1, 2, 3, 4, 5, 6, 7);
        *(bf16x8*)(wot + ((size_t)(wk * 36 + (bid & 1) * 18 + cc) << 10) + lane * 16) = af;
      }
    }
  }
  step<0, 8, false, true, true>(c, st, sc, qf, lds, 12);
  step<1, 6, false, false, true>(c, st, sc, qf, lds, 13);
  step<2, 2, false, false, true>(c, st, sc, qf, lds, 14);
  step<3, 0, false, false, false>(c, st, sc, qf, lds, 15);

  float lt = st.l + __shfl_xor(st.l, 32);
  char* xw = lds + wid * 8704;
  char* xp = lds + (wid ^ 4) * 8704;
  {
#pragma unroll
    for (int dd = 0; dd < 2; ++dd) {
      const f32x16 v = kg ? st.o[dd] : st.o[2 + dd];
#pragma unroll
      for (int a = 0; a < 4; ++a) {
        f32x4 w = {v[4 * a], v[4 * a + 1], v[4 * a + 2], v[4 * a + 3]};
        *(f32x4*)(xw + ((dd * 4 + a) * 64 + lane) * 16) = w;
      }
    }
    f32x2 ml = {st.m, lt};
    *(f32x2*)(xw + 8192 + lane * 8) = ml;
  }
  __syncthreads();
  f32x2 pml = *(const f32x2*)(xp + 8192 + lane * 8);
  const float mm = fmaxf(st.m, pml[0]);
  const float fa = __builtin_amdgcn_exp2f(st.m - mm), fb = __builtin_amdgcn_exp2f(pml[0] - mm);
  const float inv = 1.0f / (lt * fa + pml[1] * fb);
  const float wa = fa * inv, wb = fb * inv;
  char* stg = lds + 8 * 8704 + wid * 4608;
#pragma unroll
  for (int dd = 0; dd < 2; ++dd) {
    const f32x16 own = kg ? st.o[2 + dd] : st.o[dd];
#pragma unroll
    for (int a = 0; a < 4; ++a) {
      f32x4 pv = *(const f32x4*)(xp + ((dd * 4 + a) * 64 + lane) * 16);
      float o0 = own[4 * a] * wa + pv[0] * wb, o1 = own[4 * a + 1] * wa + pv[1] * wb;
      float o2 = own[4 * a + 2] * wa + pv[2] * wb, o3 = own[4 * a + 3] * wa + pv[3] * wb;
      u32x2 w = {cvtpk(o0, o1), cvtpk(o2, o3)};
      *(u32x2*)(stg + r * 144 + (32 * dd + 8 * a + 4 * h) * 2) = w;
    }
  }
  asm volatile("s_waitcnt lgkmcnt(0)" ::: "memory");
  __builtin_amdgcn_wave_barrier();
  {
    const int kst = (head * DH + kg * 64) >> 5;
    const int mt0 = (b * SQ + 32 * pb) >> 4;
#pragma unroll
    for (int i = 0; i < 4; ++i) {
      const int mi = i >> 1, ki = i & 1;
      u32x4 w = *(const u32x4*)(stg + (16 * mi + (lane & 15)) * 144 + ki * 64 + (lane >> 4) * 16);
      char* dst = (char*)og + ((size_t)((kst + ki) * 64 + mt0 + mi) << 10) + lane * 16;
      *(u32x4*)dst = w;
    }
  }
}

DI f32x4 mfma16(bf16x8 a, bf16x8 b, f32x4 c) { return __builtin_amdgcn_mfma_f32_16x16x32_bf16(a, b, c, 0, 0, 0); }
constexpr int OP_LDS = 98304;
__global__ __launch_bounds__(512, 2) void oproj_kernel(const char* __restrict__ og, const char* __restrict__ wot, u16* __restrict__ out) {
  extern __shared__ __attribute__((aligned(16))) char lds[];
  const int bid = blockIdx.x;
  const int xc = bid & 7, j = bid >> 3;
  const int mt = (xc & 3) * 4 + (j & 3), nt = (xc >> 2) * 6 + (j >> 2);
  const int tid = threadIdx.x, lane = tid & 63;
  const int wid = __builtin_amdgcn_readfirstlane(tid >> 6);
  const int rot = ((j >> 2) + 3 * (j & 3)) & 7;
  const unsigned voff = lane * 16;
  const char* ob = og + ((size_t)((8 * wid) * 64 + 4 * mt) << 10);
  const char* wb = wot + ((size_t)((8 * wid) * 36 + 3 * nt) << 10);
  bf16x8 fb[4][4], fa[4][3];
  auto issue = [&](int s) {
    const int ks = (s >> 3) * 64 + ((s + rot) & 7);
    const char* po = ob + (size_t)ks * 65536;
    const char* pw = wb + (size_t)ks * 36864;
#pragma unroll
    for (int i = 0; i < 4; ++i) asm volatile("global_load_dwordx4 %0, %1, %2 offset:%3" : "=v"(fb[s & 3][i]) : "v"(voff), "s"(po), "i"(i * 1024) : "memory");
#pragma unroll
    for (int i = 0; i < 3; ++i) asm volatile("global_load_dwordx4 %0, %1, %2 offset:%3" : "=v"(fa[s & 3][i]) : "v"(voff), "s"(pw), "i"(i * 1024) : "memory");
  };
#pragma unroll
  for (int s = 0; s < 4; ++s) issue(s);
  f32x4 acc[4][3];
#pragma unroll
  for (int i = 0; i < 4; ++i)
#pragma unroll
    for (int c = 0; c < 3; ++c) acc[i][c] = f32x4{0.f, 0.f, 0.f, 0.f};
#pragma unroll
  for (int s = 0; s < 16; ++s) {
    const int young = 7 * ((15 - s) < 3 ? (15 - s) : 3);
    asm volatile("s_waitcnt vmcnt(%7)" : "+v"(fb[s & 3][0]), "+v"(fb[s & 3][1]), "+v"(fb[s & 3][2]), "+v"(fb[s & 3][3]), "+v"(fa[s & 3][0]), "+v"(fa[s & 3][1]), "+v"(fa[s & 3][2]) : "i"(young) : "memory");
#pragma unroll
    for (int i = 0; i < 4; ++i)
#pragma unroll
      for (int c = 0; c < 3; ++c) acc[i][c] = mfma16(fa[s & 3][c], fb[s & 3][i], acc[i][c]);
    __builtin_amdgcn_sched_barrier(0);
    if (s + 4 < 16) issue(s + 4);
    __builtin_amdgcn_sched_barrier(0);
  }
  {
    char* xs = lds + wid * 12288 + lane * 16;
#pragma unroll
    for (int i = 0; i < 4; ++i)
#pragma unroll
      for (int c = 0; c < 3; ++c) *(f32x4*)(xs + (i * 3 + c) * 1024) = acc[i][c];
  }
  __syncthreads();
#pragma unroll
  for (int it = 0; it < 2; ++it) {
    const int ci = tid + 512 * it;
    if (ci < 768) {
      f32x4 sum = *(const f32x4*)(lds + ci * 16);
#pragma unroll
      for (int w = 1; w < 8; ++w) sum += *(const f32x4*)(lds + w * 12288 + ci * 16);
      const int tl = ci >> 6, ln = ci & 63, mi = tl / 3, c = tl - mi * 3;
      u32x2 w2 = {cvtpk(sum[0], sum[1]), cvtpk(sum[2], sum[3])};
      *(u32x2*)(out + (size_t)(64 * mt + 16 * mi + (ln & 15)) * DM + 48 * nt + 16 * c + 4 * (ln >> 4)) = w2;
    }
  }
}

extern "C" void kernel_launch(void* const* d_in, const int* in_sizes, int n_in, void* d_out, int out_size, void* d_ws, size_t ws_size,
                              hipStream_t stream) {
  const u16* x = (const u16*)d_in[0];
  const u16* kc = (const u16*)d_in[1];
  const u16* vc = (const u16*)d_in[2];
  const u16* wq = (const u16*)d_in[3];
  const u16* wo = (const u16*)d_in[4];
  u16* out = (u16*)d_out;
  u16* qws = (u16*)d_ws;
  u16* ows = qws + (size_t)MROWS * QW;
  hipFuncSetAttribute((const void*)attn_kernel, hipFuncAttributeMaxDynamicSharedMemorySize, ATT_LDS);
  char* wot = (char*)d_ws + (24 << 20);
  attn_kernel<<<256, 512, ATT_LDS, stream>>>(x, wq, kc, vc, ows, wo, wot);
  hipFuncSetAttribute((const void*)oproj_kernel, hipFuncAttributeMaxDynamicSharedMemorySize, OP_LDS);
  oproj_kernel<<<192, 512, OP_LDS, stream>>>((const char*)ows, wot, out);
}
```

```cpp
#include <hip/hip_runtime.h>
#include <hip/hip_bf16.h>
#include <stdint.h>

typedef __attribute__((ext_vector_type(8))) short bf16x8;
typedef __attribute__((ext_vector_type(4))) short s16x4;
typedef __attribute__((ext_vector_type(16))) float f32x16;
typedef __attribute__((ext_vector_type(4))) float f32x4;
typedef __attribute__((ext_vector_type(2))) float f32x2;
typedef __attribute__((ext_vector_type(4))) unsigned u32x4;
typedef __attribute__((ext_vector_type(2))) unsigned u32x2;
typedef __attribute__((ext_vector_type(2))) __bf16 bf16x2_t;
typedef unsigned short u16;
typedef __attribute__((address_space(3))) s16x4* lds_s16x4_p;

#define DI __device__ __forceinline__

constexpr int NB = 16, SQ = 64, DM = 576, HQ = 32, HKV = 16, SK = 1024, DH = 128;
constexpr int MROWS = NB * SQ;
constexpr int QW = HQ * DH;

DI unsigned cvtpk(float lo, float hi) {
  f32x2 v = {lo, hi};
  bf16x2_t b = __builtin_convertvector(v, bf16x2_t);
  return __builtin_bit_cast(unsigned, b);
}
DI s16x4 tr_read(const char* p) {
  return __builtin_amdgcn_ds_read_tr16_b64_v4i16((lds_s16x4_p)(p));
}
DI f32x16 mfma32(bf16x8 a, bf16x8 b, f32x16 c) { return __builtin_amdgcn_mfma_f32_32x32x16_bf16(a, b, c, 0, 0, 0); }

constexpr int ATT_LDS = 163840;
constexpr float SC = 0.08838834764831845f * 1.4426950408889634f;

DI void dma16(unsigned ldsaddr, unsigned voff, const void* sbase) {
  unsigned keep;
  asm volatile("s_mov_b32 %0, m0\n\ts_mov_b32 m0, %1\n\ts_nop 0\n\tglobal_load_lds_dwordx4 %2, %3 nt\n\ts_mov_b32 m0, %0"
               : "=&s"(keep) : "s"(ldsaddr), "v"(voff), "s"(sbase) : "memory");
}

DI void dma16p(unsigned ldsaddr, unsigned voff, const void* sbase) {
  unsigned keep;
  asm volatile("s_mov_b32 %0, m0\n\ts_mov_b32 m0, %1\n\ts_nop 0\n\tglobal_load_lds_dwordx4 %2, %3\n\ts_mov_b32 m0, %0"
               : "=&s"(keep) : "s"(ldsaddr), "v"(voff), "s"(sbase) : "memory");
}

struct AttnState {
  f32x16 o[4];
  f32x16 negm;
  float m, l;
};

template <int N> DI void wait_vm() { asm volatile("s_waitcnt vmcnt(%0)" ::"i"(N) : "memory"); }

struct AttnCtx {
  const char* kbase;
  const char* vbase;
  unsigned koff[2], voff[2];
  unsigned ldsw;
  unsigned kaddr[8];
  unsigned vaddr[8];
};


#define SBAR() __builtin_amdgcn_sched_barrier(0)
constexpr float THR = 8.0f;
template <int SL> DI void issue_k(const AttnCtx& c, int t) {
  const char* kb = c.kbase + (size_t)t * 16384;
  unsigned l0 = SL * 16384 + c.ldsw;
  dma16(l0, c.koff[0], kb);
  dma16(l0 + 1024, c.koff[1], kb);
}
template <int SL> DI void issue_v(const AttnCtx& c, int t) {
  const char* vb = c.vbase + (size_t)t * 16384;
  unsigned l0 = 65536 + SL * 16384 + c.ldsw;
  dma16(l0, c.voff[0], vb);
  dma16(l0 + 1024, c.voff[1], vb);
}
DI float rowmax16(const f32x16& s) {
  float pm = s[0];
#pragma unroll
  for (int i = 1; i < 16; ++i) pm = fmaxf(pm, s[i]);
  auto rr = __builtin_amdgcn_permlane32_swap(__float_as_uint(pm), __float_as_uint(pm), false, false);
  return fmaxf(__uint_as_float(rr[0]), __uint_as_float(rr[1]));
}
template <int SL, int WAITN, bool ISSUEK, bool ISSUEV, bool NEXT> DI void step(const AttnCtx& c, AttnState& st, f32x16& sc, const bf16x8* qf, const char* lds, int t) {
  wait_vm<WAITN>();
  asm volatile("s_waitcnt lgkmcnt(0)" ::: "memory");
  __builtin_amdgcn_s_barrier();
  SBAR();
  const char* Ks = lds + ((SL + 1) & 3) * 16384;
  const char* Vs = lds + SL * 16384;
  bf16x8 kf[8];
  if (NEXT) {
#pragma unroll
    for (int i = 0; i < 8; ++i) kf[i] = *(const bf16x8*)(Ks + c.kaddr[i]);
  }
  SBAR();
  if (ISSUEK) issue_k<SL>(c, t + 4);
  if (ISSUEV) issue_v<(SL + 3) & 3>(c, t + 3);
  SBAR();
  f32x16 sn;
  if (NEXT) {
    asm("v_mfma_f32_32x32x16_bf16 %0, %1, %2, %3" : "=&v"(sn) : "v"(kf[0]), "v"(qf[0]), "v"(st.negm));
#pragma unroll
    for (int i = 1; i < 8; ++i) sn = mfma32(kf[i], qf[i], sn);
  }
  s16x4 vlo[4][2], vhi[4][2];
#pragma unroll
  for (int k = 0; k < 2; ++k)
#pragma unroll
    for (int d = 0; d < 4; ++d) {
      vlo[d][k] = tr_read(Vs + c.vaddr[2 * d] + k * 4096);
      vhi[d][k] = tr_read(Vs + c.vaddr[2 * d + 1] + k * 4096);
    }
  float ps = 0.f;
#pragma unroll
  for (int i = 0; i < 16; ++i) { sc[i] = __builtin_amdgcn_exp2f(sc[i]); ps += sc[i]; }
  st.l += ps;
  bf16x8 pf[2];
#pragma unroll
  for (int k = 0; k < 2; ++k) {
    u32x4 w = {cvtpk(sc[8 * k], sc[8 * k + 1]), cvtpk(sc[8 * k + 2], sc[8 * k + 3]), cvtpk(sc[8 * k + 4], sc[8 * k + 5]), cvtpk(sc[8 * k + 6], sc[8 * k + 7])};
    pf[k] = __builtin_bit_cast(bf16x8, w);
  }
  if (NEXT) {
#pragma unroll
    for (int i = 0; i < 8; ++i) {
      __builtin_amdgcn_sched_group_barrier(0x8, 1, 0);
      __builtin_amdgcn_sched_group_barrier(0x100, 2, 0);
      __builtin_amdgcn_sched_group_barrier(0x2, 5, 0);
    }
  }
  SBAR();
#pragma unroll
  for (int k = 0; k < 2; ++k) {
#pragma unroll
    for (int d = 0; d < 4; ++d) {
      bf16x8 vf = __builtin_shufflevector(vlo[d][k], vhi[d][k], 0, 1, 2, 3, 4, 5, 6, 7);
      st.o[d] = mfma32(vf, pf[k], st.o[d]);
    }
  }
  if (NEXT) {
    const float pm = rowmax16(sn);
    SBAR();
    if (__builtin_expect(__any(pm > THR), 0)) {
      const float dl = fmaxf(pm, 0.f);
      const float alpha = __builtin_amdgcn_exp2f(-dl);
      st.m += dl;
      st.l *= alpha;
#pragma unroll
      for (int i = 0; i < 16; ++i) { sn[i] -= dl; st.negm[i] = -st.m; }
#pragma unroll
      for (int d = 0; d < 4; ++d)
#pragma unroll
        for (int i = 0; i < 16; ++i) st.o[d][i] *= alpha;
    }
    sc = sn;
  }
  SBAR();
}

__global__ __launch_bounds__(512, 2) void attn_kernel(const u16* __restrict__ x, const u16* __restrict__ wq, const u16* __restrict__ kc,
                                                      const u16* __restrict__ vc, u16* __restrict__ og,
                                                      const u16* __restrict__ wo, char* __restrict__ wot) {
  extern __shared__ __attribute__((aligned(16))) char lds[];
  const int bid = blockIdx.x;
  const int hkv = 2 * (bid & 7) + ((bid >> 3) & 1), b = bid >> 4;
  const int rot = 2 * ((b * 9) >> 4);
  const int tid = threadIdx.x, lane = tid & 63, r = lane & 31, h = lane >> 5;
  const int wid = __builtin_amdgcn_readfirstlane(tid >> 6);
  const int kg = wid >> 2, g = (wid >> 1) & 1, pb = wid & 1;
  const int head = hkv * 2 + g;

  AttnCtx c;
  c.kbase = (const char*)(kc + (size_t)(b * HKV + hkv) * SK * DH);
  c.vbase = (const char*)(vc + (size_t)(b * HKV + hkv) * SK * DH);
  c.ldsw = wid * 8 * 256;
#pragma unroll
  for (int i = 0; i < 2; ++i) {
    const int row = 8 * wid + 4 * i + (lane >> 4), pos = lane & 15;
    c.koff[i] = row * 256 + ((pos ^ (row & 15)) << 4);
    c.voff[i] = row * 256 + ((pos ^ (((row & 3) << 2) | ((row >> 2) & 3))) << 4);
  }
  {
    const int krow = 32 * kg + r;
#pragma unroll
    for (int i = 0; i < 8; ++i) c.kaddr[i] = krow * 256 + (((2 * i + h) ^ (r & 15)) << 4);
    const int blk = (lane >> 4) & 1, q4 = (lane & 15) >> 2, p4 = lane & 3;
#pragma unroll
    for (int d = 0; d < 4; ++d)
#pragma unroll
      for (int t = 0; t < 2; ++t) {
        const int row = 32 * kg + 8 * t + 4 * h + q4;
        const int ch = 4 * d + 2 * blk + (p4 >> 1);
        const int sw = ((row & 3) << 2) | ((row >> 2) & 3);
        c.vaddr[2 * d + t] = 65536 + row * 256 + ((ch ^ sw) << 4) + 8 * (p4 & 1);
        asm volatile("" : "+v"(c.vaddr[2 * d + t]));
      }
  }
  const char* wqb = (const char*)(wq + (size_t)hkv * 256);
  const char* xb = (const char*)(x + (size_t)b * SQ * DM);
  constexpr unsigned XB0 = 131072;
  unsigned wsrc[2], xsrc;
#pragma unroll
  for (int i = 0; i < 2; ++i) {
    const int row = 4 * wid + 2 * i + (lane >> 5), pos = lane & 31;
    const int f = ((row & 3) << 2) | ((row >> 2) & 3);
    wsrc[i] = row * 8192 + ((pos ^ f) << 4);
  }
  {
    const int row = 8 * wid + (lane >> 3), pos = lane & 7;
    xsrc = row * 1152 + ((pos ^ ((row >> 1) & 7)) << 4);
  }
  auto issue_w = [&](int chi, unsigned wbuf) {
    int ch = chi + rot; if (ch >= 18) ch -= 18;
    const char* wsb = wqb + (size_t)ch * 32 * 8192;
#pragma unroll
    for (int i = 0; i < 2; ++i) dma16p(wbuf + wid * 2048 + i * 1024, wsrc[i], wsb);
  };
  auto issue_x = [&](int p) {
    int ch = 2 * p + rot; if (ch >= 18) ch -= 18;
    dma16p(XB0 + (p & 3) * 8192 + wid * 1024, xsrc, xb + ch * 64);
  };
  constexpr unsigned PS[8] = {6, 7, 0, 1, 4, 2, 5, 3};
#pragma unroll
  for (int p = 0; p < 4; ++p) { issue_w(2 * p, PS[2 * p] * 16384); issue_w(2 * p + 1, PS[2 * p + 1] * 16384); issue_x(p); }
  unsigned waddr[2][2], xaddr[4];
  {
    const int blk = (lane >> 4) & 1, q4 = (lane & 15) >> 2, p4 = lane & 3;
#pragma unroll
    for (int j = 0; j < 2; ++j)
#pragma unroll
      for (int t = 0; t < 2; ++t) {
        const int row = 8 * h + 4 * t + q4;
        const int chn = 4 * (4 * g + 2 * kg + j) + 2 * blk + (p4 >> 1);
        const int f = ((row & 3) << 2) | ((row >> 2) & 3);
        waddr[j][t] = row * 512 + ((chn ^ f) << 4) + 8 * (p4 & 1);
      }
    const int row = 32 * pb + r;
#pragma unroll
    for (int s = 0; s < 4; ++s) xaddr[s] = row * 128 + (((2 * s + h) ^ ((row >> 1) & 7)) << 4);
  }
  f32x16 qa[2] = {};
  auto proj_chunk = [&](unsigned wbuf, unsigned xbuf, int e) {
#pragma unroll
    for (int s = 0; s < 2; ++s) {
      const bf16x8 xf = *(const bf16x8*)(lds + xbuf + xaddr[2 * e + s]);
#pragma unroll
      for (int j = 0; j < 2; ++j) {
        s16x4 lo = tr_read(lds + wbuf + s * 8192 + waddr[j][0]);
        s16x4 hi = tr_read(lds + wbuf + s * 8192 + waddr[j][1]);
        qa[j] = mfma32(__builtin_shufflevector(lo, hi, 0, 1, 2, 3, 4, 5, 6, 7), xf, qa[j]);
      }
    }
  };
  constexpr int PW[18] = {15, 15, 12, 15, 12, 15, 12, 15, 14, 15, 14, 15, 14, 14, 13, 13, 12, 12};
#pragma unroll
  for (int ch = 0; ch < 18; ++ch) {
    {
      asm volatile("s_waitcnt vmcnt(%0)" ::"i"(PW[ch]) : "memory");
    }
    asm volatile("s_waitcnt lgkmcnt(0)" ::: "memory");
    __builtin_amdgcn_s_barrier();
    if (ch >= 2 && (ch & 1) == 0 && ch / 2 + 3 <= 8) issue_x(ch / 2 + 3);
    if (ch >= 1 && ch <= 10) issue_w(ch + 7, PS[(ch + 7) & 7] * 16384);
    if (ch == 11) issue_k<0>(c, 0);
    if (ch == 12) issue_k<1>(c, 1);
    if (ch == 13) issue_v<0>(c, 0);
    if (ch == 14) issue_k<2>(c, 2);
    if (ch == 15) issue_v<1>(c, 1);
    if (ch == 16) issue_k<3>(c, 3);
    if (ch == 17) issue_v<2>(c, 2);
    proj_chunk(PS[ch & 7] * 16384, XB0 + ((ch >> 1) & 3) * 8192, ch & 1);
  }
  asm volatile("s_waitcnt lgkmcnt(0)" ::: "memory");
  __builtin_amdgcn_s_barrier();
  {
    char* qt = lds + 131072 + (g * 2 + pb) * 8192 + r * 256;
#pragma unroll
    for (int j = 0; j < 2; ++j)
#pragma unroll
      for (int a = 0; a < 4; ++a) {
        unsigned w0 = cvtpk(qa[j][4 * a], qa[j][4 * a + 1]), w1 = cvtpk(qa[j][4 * a + 2], qa[j][4 * a + 3]);
        w0 = cvtpk(__uint_as_float(w0 << 16) * SC, __uint_as_float(w0 & 0xffff0000u) * SC);
        w1 = cvtpk(__uint_as_float(w1 << 16) * SC, __uint_as_float(w1 & 0xffff0000u) * SC);
        u32x2 w = {w0, w1};
        *(u32x2*)(qt + (((8 * kg + 4 * j + a) ^ (r & 15)) << 4) + 8 * h) = w;
      }
  }
  wait_vm<12>();
  asm volatile("s_waitcnt lgkmcnt(0)" ::: "memory");
  __builtin_amdgcn_s_barrier();
  bf16x8 qf[8];
  {
    const char* qt = lds + 131072 + (g * 2 + pb) * 8192 + r * 256;
#pragma unroll
    for (int i = 0; i < 8; ++i) qf[i] = *(const bf16x8*)(qt + (((2 * i + h) ^ (r & 15)) << 4));
  }

  AttnState st;
#pragma unroll
  for (int d = 0; d < 4; ++d) st.o[d] = f32x16{};
  st.m = -1e30f; st.l = 0.f;

  f32x16 sc = {};
#pragma unroll
  for (int i = 0; i < 8; ++i) sc = mfma32(*(const bf16x8*)(lds + c.kaddr[i]), qf[i], sc);
  st.m = rowmax16(sc);
#pragma unroll
  for (int i = 0; i < 16; ++i) { sc[i] -= st.m; st.negm[i] = -st.m; }
  step<0, 8, true, true, true>(c, st, sc, qf, lds, 0);
  step<1, 8, true, true, true>(c, st, sc, qf, lds, 1);
  {
    const char* wsl = (const char*)(wo + (size_t)(32 * (bid >> 1)) * DM + 288 * (bid & 1));
#pragma unroll
    for (int i = 0; i < 3; ++i) {
      const int p = wid + 8 * i;
      const int idx = p * 64 + lane, row = idx / 36, chn = idx - row * 36;
      dma16(131072 + p * 1024, p < 18 ? (unsigned)(row * 1152 + chn * 16) : 0u, wsl);
    }
  }
  step<2, 8, true, true, true>(c, st, sc, qf, lds, 2);
  step<3, 8, true, true, true>(c, st, sc, qf, lds, 3);
#pragma unroll 1
  for (int t = 4; t < 12; t += 4) {
    step<0, 8, true, true, true>(c, st, sc, qf, lds, t);
    step<1, 8, true, true, true>(c, st, sc, qf, lds, t + 1);
    step<2, 8, true, true, true>(c, st, sc, qf, lds, t + 2);
    step<3, 8, true, true, true>(c, st, sc, qf, lds, t + 3);
  }
  {
    const int wk = bid >> 1;
    const int g4 = lane >> 4, q4 = (lane & 15) >> 2, p4 = lane & 3;
    const char* sb = lds + 131072 + (8 * g4 + q4) * 576 + p4 * 8;
#pragma unroll
    for (int i = 0; i < 3; ++i) {
      const int cc = wid + 8 * i;
      if (cc < 18) {
        s16x4 lo = tr_read(sb + cc * 32), hi = tr_read(sb + cc * 32 + 4 * 576);
        bf16x8 af = __builtin_shufflevector(lo, hi, 0, 1, 2, 3, 4, 5, 6, 7);
        *(bf16x8*)(wot + ((size_t)(wk * 36 + (bid & 1) * 18 + cc) << 10) + lane * 16) = af;
      }
    }
  }
  step<0, 8, false, true, true>(c, st, sc, qf, lds, 12);
  step<1, 6, false, false, true>(c, st, sc, qf, lds, 13);
  step<2, 2, false, false, true>(c, st, sc, qf, lds, 14);
  step<3, 0, false, false, false>(c, st, sc, qf, lds, 15);

  float lt = st.l + __shfl_xor(st.l, 32);
  char* xw = lds + wid * 8704;
  char* xp = lds + (wid ^ 4) * 8704;
  {
#pragma unroll
    for (int dd = 0; dd < 2; ++dd) {
      const f32x16 v = kg ? st.o[dd] : st.o[2 + dd];
#pragma unroll
      for (int a = 0; a < 4; ++a) {
        f32x4 w = {v[4 * a], v[4 * a + 1], v[4 * a + 2], v[4 * a + 3]};
        *(f32x4*)(xw + ((dd * 4 + a) * 64 + lane) * 16) = w;
      }
    }
    f32x2 ml = {st.m, lt};
    *(f32x2*)(xw + 8192 + lane * 8) = ml;
  }
  __syncthreads();
  f32x2 pml = *(const f32x2*)(xp + 8192 + lane * 8);
  const float mm = fmaxf(st.m, pml[0]);
  const float fa = __builtin_amdgcn_exp2f(st.m - mm), fb = __builtin_amdgcn_exp2f(pml[0] - mm);
  const float inv = 1.0f / (lt * fa + pml[1] * fb);
  const float wa = fa * inv, wb = fb * inv;
  char* stg = lds + 8 * 8704 + wid * 4608;
#pragma unroll
  for (int dd = 0; dd < 2; ++dd) {
    const f32x16 own = kg ? st.o[2 + dd] : st.o[dd];
#pragma unroll
    for (int a = 0; a < 4; ++a) {
      f32x4 pv = *(const f32x4*)(xp + ((dd * 4 + a) * 64 + lane) * 16);
      float o0 = own[4 * a] * wa + pv[0] * wb, o1 = own[4 * a + 1] * wa + pv[1] * wb;
      float o2 = own[4 * a + 2] * wa + pv[2] * wb, o3 = own[4 * a + 3] * wa + pv[3] * wb;
      u32x2 w = {cvtpk(o0, o1), cvtpk(o2, o3)};
      *(u32x2*)(stg + r * 144 + (32 * dd + 8 * a + 4 * h) * 2) = w;
    }
  }
  asm volatile("s_waitcnt lgkmcnt(0)" ::: "memory");
  __builtin_amdgcn_wave_barrier();
  {
    const int kst = (head * DH + kg * 64) >> 5;
    const int mt0 = (b * SQ + 32 * pb) >> 4;
#pragma unroll
    for (int i = 0; i < 4; ++i) {
      const int mi = i >> 1, ki = i & 1;
      u32x4 w = *(const u32x4*)(stg + (16 * mi + (lane & 15)) * 144 + ki * 64 + (lane >> 4) * 16);
      char* dst = (char*)og + ((size_t)((kst + ki) * 64 + mt0 + mi) << 10) + lane * 16;
      *(u32x4*)dst = w;
    }
  }
}

DI f32x4 mfma16(bf16x8 a, bf16x8 b, f32x4 c) { return __builtin_amdgcn_mfma_f32_16x16x32_bf16(a, b, c, 0, 0, 0); }
constexpr int OP_LDS = 98304;
__global__ __launch_bounds__(512, 2) void oproj_kernel(const char* __restrict__ og, const char* __restrict__ wot, u16* __restrict__ out) {
  extern __shared__ __attribute__((aligned(16))) char lds[];
  const int bid = blockIdx.x;
  const int xc = bid & 7, j = bid >> 3;
  const int mt = (xc & 3) * 4 + (j & 3), nt = (xc >> 2) * 6 + (j >> 2);
  const int tid = threadIdx.x, lane = tid & 63;
  const int wid = __builtin_amdgcn_readfirstlane(tid >> 6);
  const int rot = ((j >> 2) + 3 * (j & 3)) & 7;
  const unsigned voff = lane * 16;
  const char* ob = og + ((size_t)((8 * wid) * 64 + 4 * mt) << 10);
  const char* wb = wot + ((size_t)((8 * wid) * 36 + 3 * nt) << 10);
  bf16x8 fb[4][4], fa[4][3];
  auto issue = [&](int s) {
    const int ks = (s >> 3) * 64 + ((s + rot) & 7);
    const char* po = ob + (size_t)ks * 65536;
    const char* pw = wb + (size_t)ks * 36864;
#pragma unroll
    for (int i = 0; i < 4; ++i) asm volatile("global_load_dwordx4 %0, %1, %2 offset:%3" : "=v"(fb[s & 3][i]) : "v"(voff), "s"(po), "i"(i * 1024) : "memory");
#pragma unroll
    for (int i = 0; i < 3; ++i) asm volatile("global_load_dwordx4 %0, %1, %2 offset:%3" : "=v"(fa[s & 3][i]) : "v"(voff), "s"(pw), "i"(i * 1024) : "memory");
  };
#pragma unroll
  for (int s = 0; s < 4; ++s) issue(s);
  f32x4 acc[4][3];
#pragma unroll
  for (int i = 0; i < 4; ++i)
#pragma unroll
    for (int c = 0; c < 3; ++c) acc[i][c] = f32x4{0.f, 0.f, 0.f, 0.f};
#pragma unroll
  for (int s = 0; s < 16; ++s) {
    const int young = 7 * ((15 - s) < 3 ? (15 - s) : 3);
    asm volatile("s_waitcnt vmcnt(%7)" : "+v"(fb[s & 3][0]), "+v"(fb[s & 3][1]), "+v"(fb[s & 3][2]), "+v"(fb[s & 3][3]), "+v"(fa[s & 3][0]), "+v"(fa[s & 3][1]), "+v"(fa[s & 3][2]) : "i"(young) : "memory");
#pragma unroll
    for (int i = 0; i < 4; ++i)
#pragma unroll
      for (int c = 0; c < 3; ++c) acc[i][c] = mfma16(fa[s & 3][c], fb[s & 3][i], acc[i][c]);
    __builtin_amdgcn_sched_barrier(0);
    if (s + 4 < 16) issue(s + 4);
    __builtin_amdgcn_sched_barrier(0);
  }
  {
    char* xs = lds + wid * 12288 + lane * 16;
#pragma unroll
    for (int i = 0; i < 4; ++i)
#pragma unroll
      for (int c = 0; c < 3; ++c) *(f32x4*)(xs + (i * 3 + c) * 1024) = acc[i][c];
  }
  __syncthreads();
#pragma unroll
  for (int it = 0; it < 2; ++it) {
    const int ci = tid + 512 * it;
    if (ci < 768) {
      f32x4 sum = *(const f32x4*)(lds + ci * 16);
#pragma unroll
      for (int w = 1; w < 8; ++w) sum += *(const f32x4*)(lds + w * 12288 + ci * 16);
      const int tl = ci >> 6, ln = ci & 63, mi = tl / 3, c = tl - mi * 3;
      u32x2 w2 = {cvtpk(sum[0], sum[1]), cvtpk(sum[2], sum[3])};
      *(u32x2*)(out + (size_t)(64 * mt + 16 * mi + (ln & 15)) * DM + 48 * nt + 16 * c + 4 * (ln >> 4)) = w2;
    }
  }
}

extern "C" void kernel_launch(void* const* d_in, const int* in_sizes, int n_in, void* d_out, int out_size, void* d_ws, size_t ws_size,
                              hipStream_t stream) {
  const u16* x = (const u16*)d_in[0];
  const u16* kc = (const u16*)d_in[1];
  const u16* vc = (const u16*)d_in[2];
  const u16* wq = (const u16*)d_in[3];
  const u16* wo = (const u16*)d_in[4];
  u16* out = (u16*)d_out;
  u16* ows = (u16*)((char*)d_ws + (8 << 20));
  char* wot = (char*)d_ws + (24 << 20);
  (void)hipFuncSetAttribute((const void*)attn_kernel, hipFuncAttributeMaxDynamicSharedMemorySize, ATT_LDS);
  attn_kernel<<<256, 512, ATT_LDS, stream>>>(x, wq, kc, vc, ows, wo, wot);
  (void)hipFuncSetAttribute((const void*)oproj_kernel, hipFuncAttributeMaxDynamicSharedMemorySize, OP_LDS);
  oproj_kernel<<<192, 512, OP_LDS, stream>>>((const char*)ows, wot, out);
}
```

```cpp
#include <hip/hip_runtime.h>
#include <hip/hip_bf16.h>
#include <stdint.h>

typedef __attribute__((ext_vector_type(8))) short bf16x8;
typedef __attribute__((ext_vector_type(4))) short s16x4;
typedef __attribute__((ext_vector_type(16))) float f32x16;
typedef __attribute__((ext_vector_type(4))) float f32x4;
typedef __attribute__((ext_vector_type(2))) float f32x2;
typedef __attribute__((ext_vector_type(4))) unsigned u32x4;
typedef __attribute__((ext_vector_type(2))) unsigned u32x2;
typedef __attribute__((ext_vector_type(2))) __bf16 bf16x2_t;
typedef unsigned short u16;
typedef __attribute__((address_space(3))) s16x4* lds_s16x4_p;

#define DI __device__ __forceinline__

constexpr int NB = 16, SQ = 64, DM = 576, HQ = 32, HKV = 16, SK = 1024, DH = 128;
constexpr int MROWS = NB * SQ;
constexpr int QW = HQ * DH;

DI unsigned cvtpk(float lo, float hi) {
  f32x2 v = {lo, hi};
  bf16x2_t b = __builtin_convertvector(v, bf16x2_t);
  return __builtin_bit_cast(unsigned, b);
}
DI s16x4 tr_read(const char* p) {
  return __builtin_amdgcn_ds_read_tr16_b64_v4i16((lds_s16x4_p)(p));
}
DI f32x16 mfma32(bf16x8 a, bf16x8 b, f32x16 c) { return __builtin_amdgcn_mfma_f32_32x32x16_bf16(a, b, c, 0, 0, 0); }

constexpr int ATT_LDS = 163840;
constexpr float SC = 0.08838834764831845f * 1.4426950408889634f;

DI void dma16(unsigned ldsaddr, unsigned voff, const void* sbase) {
  unsigned keep;
  asm volatile("s_mov_b32 %0, m0\n\ts_mov_b32 m0, %1\n\ts_nop 0\n\tglobal_load_lds_dwordx4 %2, %3 nt\n\ts_mov_b32 m0, %0"
               : "=&s"(keep) : "s"(ldsaddr), "v"(voff), "s"(sbase) : "memory");
}

DI void dma16p(unsigned ldsaddr, unsigned voff, const void* sbase) {
  unsigned keep;
  asm volatile("s_mov_b32 %0, m0\n\ts_mov_b32 m0, %1\n\ts_nop 0\n\tglobal_load_lds_dwordx4 %2, %3\n\ts_mov_b32 m0, %0"
               : "=&s"(keep) : "s"(ldsaddr), "v"(voff), "s"(sbase) : "memory");
}

struct AttnState {
  f32x16 o[4];
  f32x16 negm;
  float m, l;
};

template <int N> DI void wait_vm() { asm volatile("s_waitcnt vmcnt(%0)" ::"i"(N) : "memory"); }

struct AttnCtx {
  const char* kbase;
  const char* vbase;
  unsigned koff[2], voff[2];
  unsigned ldsw;
  unsigned kaddr[8];
  unsigned vaddr[8];
};


#define SBAR() __builtin_amdgcn_sched_barrier(0)
constexpr float THR = 8.0f;
template <int SL> DI void issue_k(const AttnCtx& c, int t) {
  const char* kb = c.kbase + (size_t)t * 16384;
  unsigned l0 = SL * 16384 + c.ldsw;
  dma16(l0, c.koff[0], kb);
  dma16(l0 + 1024, c.koff[1], kb);
}
template <int SL> DI void issue_v(const AttnCtx& c, int t) {
  const char* vb = c.vbase + (size_t)t * 16384;
  unsigned l0 = 65536 + SL * 16384 + c.ldsw;
  dma16(l0, c.voff[0], vb);
  dma16(l0 + 1024, c.voff[1], vb);
}
DI float rowmax16(const f32x16& s) {
  float pm = s[0];
#pragma unroll
  for (int i = 1; i < 16; ++i) pm = fmaxf(pm, s[i]);
  auto rr = __builtin_amdgcn_permlane32_swap(__float_as_uint(pm), __float_as_uint(pm), false, false);
  return fmaxf(__uint_as_float(rr[0]), __uint_as_float(rr[1]));
}
template <int SL, int WAITN, bool ISSUEK, bool ISSUEV, bool NEXT> DI void step(const AttnCtx& c, AttnState& st, f32x16& sc, const bf16x8* qf, const char* lds, int t) {
  wait_vm<WAITN>();
  asm volatile("s_waitcnt lgkmcnt(0)" ::: "memory");
  __builtin_amdgcn_s_barrier();
  SBAR();
  const char* Ks = lds + ((SL + 1) & 3) * 16384;
  const char* Vs = lds + SL * 16384;
  const char* kb = c.kbase + (size_t)(t + 4) * 16384;
  const char* vb = c.vbase + (size_t)(t + 3) * 16384;
  const unsigned lk = SL * 16384 + c.ldsw, lv = 65536 + ((SL + 3) & 3) * 16384 + c.ldsw;
  bf16x8 kf[8];
  s16x4 vlo[4][2], vhi[4][2];
  if (NEXT) {
#pragma unroll
    for (int i = 0; i < 8; ++i) kf[i] = *(const bf16x8*)(Ks + c.kaddr[i]);
  } else {
#pragma unroll
    for (int d = 0; d < 4; ++d) { vlo[d][0] = tr_read(Vs + c.vaddr[2 * d]); vhi[d][0] = tr_read(Vs + c.vaddr[2 * d + 1]); }
  }
  SBAR();
  float ps0, ps1;
#pragma unroll
  for (int i = 0; i < 8; ++i) sc[i] = __builtin_amdgcn_exp2f(sc[i]);
  SBAR();
  f32x16 sn;
  bf16x8 pf[2];
  if (NEXT) asm("v_mfma_f32_32x32x16_bf16 %0, %1, %2, %3" : "=&v"(sn) : "v"(kf[0]), "v"(qf[0]), "v"(st.negm));
#pragma unroll
  for (int i = 8; i < 12; ++i) sc[i] = __builtin_amdgcn_exp2f(sc[i]);
  SBAR();
  if (NEXT) sn = mfma32(kf[1], qf[1], sn);
#pragma unroll
  for (int i = 12; i < 16; ++i) sc[i] = __builtin_amdgcn_exp2f(sc[i]);
  SBAR();
  if (NEXT) sn = mfma32(kf[2], qf[2], sn);
  SBAR();
  if (ISSUEK) dma16(lk, c.koff[0], kb);
  SBAR();
  if (NEXT) sn = mfma32(kf[3], qf[3], sn);
  if (NEXT) {
#pragma unroll
    for (int d = 0; d < 4; ++d) { vlo[d][0] = tr_read(Vs + c.vaddr[2 * d]); vhi[d][0] = tr_read(Vs + c.vaddr[2 * d + 1]); }
  }
  SBAR();
  if (NEXT) sn = mfma32(kf[4], qf[4], sn);
  {
    u32x4 w = {cvtpk(sc[0], sc[1]), cvtpk(sc[2], sc[3]), cvtpk(sc[4], sc[5]), cvtpk(sc[6], sc[7])};
    pf[0] = __builtin_bit_cast(bf16x8, w);
    ps0 = ((sc[0] + sc[1]) + (sc[2] + sc[3])) + ((sc[4] + sc[5]) + (sc[6] + sc[7]));
  }
  SBAR();
  if (NEXT) sn = mfma32(kf[5], qf[5], sn);
  SBAR();
  if (ISSUEK) dma16(lk + 1024, c.koff[1], kb);
  SBAR();
  if (NEXT) sn = mfma32(kf[6], qf[6], sn);
#pragma unroll
  for (int d = 0; d < 4; ++d) { vlo[d][1] = tr_read(Vs + c.vaddr[2 * d] + 4096); vhi[d][1] = tr_read(Vs + c.vaddr[2 * d + 1] + 4096); }
  SBAR();
  if (NEXT) sn = mfma32(kf[7], qf[7], sn);
  {
    u32x4 w = {cvtpk(sc[8], sc[9]), cvtpk(sc[10], sc[11]), cvtpk(sc[12], sc[13]), cvtpk(sc[14], sc[15])};
    pf[1] = __builtin_bit_cast(bf16x8, w);
    ps1 = ((sc[8] + sc[9]) + (sc[10] + sc[11])) + ((sc[12] + sc[13]) + (sc[14] + sc[15]));
  }
  SBAR();
#define PVM(d, k) st.o[d] = mfma32(__builtin_shufflevector(vlo[d][k], vhi[d][k], 0, 1, 2, 3, 4, 5, 6, 7), pf[k], st.o[d])
  PVM(0, 0);
  st.l += ps0 + ps1;
  SBAR();
  PVM(1, 0);
  SBAR();
  if (ISSUEV) dma16(lv, c.voff[0], vb);
  SBAR();
  PVM(2, 0);
  float pm = 0.f;
  if (NEXT) { pm = sn[0];
#pragma unroll
    for (int i = 1; i < 8; ++i) pm = fmaxf(pm, sn[i]); }
  SBAR();
  PVM(3, 0);
  if (NEXT) {
#pragma unroll
    for (int i = 8; i < 16; ++i) pm = fmaxf(pm, sn[i]); }
  SBAR();
  PVM(0, 1);
  SBAR();
  if (ISSUEV) dma16(lv + 1024, c.voff[1], vb);
  SBAR();
  PVM(1, 1);
  if (NEXT) {
    auto rr = __builtin_amdgcn_permlane32_swap(__float_as_uint(pm), __float_as_uint(pm), false, false);
    pm = fmaxf(__uint_as_float(rr[0]), __uint_as_float(rr[1]));
  }
  SBAR();
  PVM(2, 1);
  PVM(3, 1);
#undef PVM
  SBAR();
  if (NEXT) {
    if (__builtin_expect(__any(pm > THR), 0)) {
      const float dl = fmaxf(pm, 0.f);
      const float alpha = __builtin_amdgcn_exp2f(-dl);
      st.m += dl;
      st.l *= alpha;
#pragma unroll
      for (int i = 0; i < 16; ++i) { sn[i] -= dl; st.negm[i] = -st.m; }
#pragma unroll
      for (int d = 0; d < 4; ++d)
#pragma unroll
        for (int i = 0; i < 16; ++i) st.o[d][i] *= alpha;
    }
    sc = sn;
  }
  SBAR();
}

__global__ __launch_bounds__(512, 2) void attn_kernel(const u16* __restrict__ x, const u16* __restrict__ wq, const u16* __restrict__ kc,
                                                      const u16* __restrict__ vc, u16* __restrict__ og,
                                                      const u16* __restrict__ wo, char* __restrict__ wot) {
  extern __shared__ __attribute__((aligned(16))) char lds[];
  const int bid = blockIdx.x;
  const int hkv = 2 * (bid & 7) + ((bid >> 3) & 1), b = bid >> 4;
  const int rot = 2 * ((b * 9) >> 4);
  const int tid = threadIdx.x, lane = tid & 63, r = lane & 31, h = lane >> 5;
  const int wid = __builtin_amdgcn_readfirstlane(tid >> 6);
  const int kg = wid >> 2, g = (wid >> 1) & 1, pb = wid & 1;
  const int head = hkv * 2 + g;

  AttnCtx c;
  c.kbase = (const char*)(kc + (size_t)(b * HKV + hkv) * SK * DH);
  c.vbase = (const char*)(vc + (size_t)(b * HKV + hkv) * SK * DH);
  c.ldsw = wid * 8 * 256;
#pragma unroll
  for (int i = 0; i < 2; ++i) {
    const int row = 8 * wid + 4 * i + (lane >> 4), pos = lane & 15;
    c.koff[i] = row * 256 + ((pos ^ (row & 15)) << 4);
    c.voff[i] = row * 256 + ((pos ^ (((row & 3) << 2) | ((row >> 2) & 3))) << 4);
  }
  {
    const int krow = 32 * kg + r;
#pragma unroll
    for (int i = 0; i < 8; ++i) c.kaddr[i] = krow * 256 + (((2 * i + h) ^ (r & 15)) << 4);
    const int blk = (lane >> 4) & 1, q4 = (lane & 15) >> 2, p4 = lane & 3;
#pragma unroll
    for (int d = 0; d < 4; ++d)
#pragma unroll
      for (int t = 0; t < 2; ++t) {
        const int row = 32 * kg + 8 * t + 4 * h + q4;
        const int ch = 4 * d + 2 * blk + (p4 >> 1);
        const int sw = ((row & 3) << 2) | ((row >> 2) & 3);
        c.vaddr[2 * d + t] = 65536 + row * 256 + ((ch ^ sw) << 4) + 8 * (p4 & 1);
        asm volatile("" : "+v"(c.vaddr[2 * d + t]));
      }
  }
  const char* wqb = (const char*)(wq + (size_t)hkv * 256);
  const char* xb = (const char*)(x + (size_t)b * SQ * DM);
  constexpr unsigned XB0 = 131072;
  unsigned wsrc[2], xsrc;
#pragma unroll
  for (int i = 0; i < 2; ++i) {
    const int row = 4 * wid + 2 * i + (lane >> 5), pos = lane & 31;
    const int f = ((row & 3) << 2) | ((row >> 2) & 3);
    wsrc[i] = row * 8192 + ((pos ^ f) << 4);
  }
  {
    const int row = 8 * wid + (lane >> 3), pos = lane & 7;
    xsrc = row * 1152 + ((pos ^ ((row >> 1) & 7)) << 4);
  }
  auto issue_w = [&](int chi, unsigned wbuf) {
    int ch = chi + rot; if (ch >= 18) ch -= 18;
    const char* wsb = wqb + (size_t)ch * 32 * 8192;
#pragma unroll
    for (int i = 0; i < 2; ++i) dma16p(wbuf + wid * 2048 + i * 1024, wsrc[i], wsb);
  };
  auto issue_x = [&](int p) {
    int ch = 2 * p + rot; if (ch >= 18) ch -= 18;
    dma16p(XB0 + (p & 3) * 8192 + wid * 1024, xsrc, xb + ch * 64);
  };
  constexpr unsigned PS[8] = {6, 7, 0, 1, 4, 2, 5, 3};
#pragma unroll
  for (int p = 0; p < 4; ++p) { issue_w(2 * p, PS[2 * p] * 16384); issue_w(2 * p + 1, PS[2 * p + 1] * 16384); issue_x(p); }
  unsigned waddr[2][2], xaddr[4];
  {
    const int blk = (lane >> 4) & 1, q4 = (lane & 15) >> 2, p4 = lane & 3;
#pragma unroll
    for (int j = 0; j < 2; ++j)
#pragma unroll
      for (int t = 0; t < 2; ++t) {
        const int row = 8 * h + 4 * t + q4;
        const int chn = 4 * (4 * g + 2 * kg + j) + 2 * blk + (p4 >> 1);
        const int f = ((row & 3) << 2) | ((row >> 2) & 3);
        waddr[j][t] = row * 512 + ((chn ^ f) << 4) + 8 * (p4 & 1);
      }
    const int row = 32 * pb + r;
#pragma unroll
    for (int s = 0; s < 4; ++s) xaddr[s] = row * 128 + (((2 * s + h) ^ ((row >> 1) & 7)) << 4);
  }
  f32x16 qa[2] = {};
  auto proj_chunk = [&](unsigned wbuf, unsigned xbuf, int e) {
#pragma unroll
    for (int s = 0; s < 2; ++s) {
      const bf16x8 xf = *(const bf16x8*)(lds + xbuf + xaddr[2 * e + s]);
#pragma unroll
      for (int j = 0; j < 2; ++j) {
        s16x4 lo = tr_read(lds + wbuf + s * 8192 + waddr[j][0]);
        s16x4 hi = tr_read(lds + wbuf + s * 8192 + waddr[j][1]);
        qa[j] = mfma32(__builtin_shufflevector(lo, hi, 0, 1, 2, 3, 4, 5, 6, 7), xf, qa[j]);
      }
    }
  };
  constexpr int PW[18] = {15, 15, 12, 15, 12, 15, 12, 15, 14, 15, 14, 15, 14, 14, 13, 13, 12, 12};
#pragma unroll
  for (int ch = 0; ch < 18; ++ch) {
    {
      asm volatile("s_waitcnt vmcnt(%0)" ::"i"(PW[ch]) : "memory");
    }
    asm volatile("s_waitcnt lgkmcnt(0)" ::: "memory");
    __builtin_amdgcn_s_barrier();
    if (ch >= 2 && (ch & 1) == 0 && ch / 2 + 3 <= 8) issue_x(ch / 2 + 3);
    if (ch >= 1 && ch <= 10) issue_w(ch + 7, PS[(ch + 7) & 7] * 16384);
    if (ch == 11) issue_k<0>(c, 0);
    if (ch == 12) issue_k<1>(c, 1);
    if (ch == 13) issue_v<0>(c, 0);
    if (ch == 14) issue_k<2>(c, 2);
    if (ch == 15) issue_v<1>(c, 1);
    if (ch == 16) issue_k<3>(c, 3);
    if (ch == 17) issue_v<2>(c, 2);
    proj_chunk(PS[ch & 7] * 16384, XB0 + ((ch >> 1) & 3) * 8192, ch & 1);
  }
  asm volatile("s_waitcnt lgkmcnt(0)" ::: "memory");
  __builtin_amdgcn_s_barrier();
  {
    char* qt = lds + 131072 + (g * 2 + pb) * 8192 + r * 256;
#pragma unroll
    for (int j = 0; j < 2; ++j)
#pragma unroll
      for (int a = 0; a < 4; ++a) {
        unsigned w0 = cvtpk(qa[j][4 * a], qa[j][4 * a + 1]), w1 = cvtpk(qa[j][4 * a + 2], qa[j][4 * a + 3]);
        w0 = cvtpk(__uint_as_float(w0 << 16) * SC, __uint_as_float(w0 & 0xffff0000u) * SC);
        w1 = cvtpk(__uint_as_float(w1 << 16) * SC, __uint_as_float(w1 & 0xffff0000u) * SC);
        u32x2 w = {w0, w1};
        *(u32x2*)(qt + (((8 * kg + 4 * j + a) ^ (r & 15)) << 4) + 8 * h) = w;
      }
  }
  wait_vm<12>();
  asm volatile("s_waitcnt lgkmcnt(0)" ::: "memory");
  __builtin_amdgcn_s_barrier();
  bf16x8 qf[8];
  {
    const char* qt = lds + 131072 + (g * 2 + pb) * 8192 + r * 256;
#pragma unroll
    for (int i = 0; i < 8; ++i) qf[i] = *(const bf16x8*)(qt + (((2 * i + h) ^ (r & 15)) << 4));
  }

  AttnState st;
#pragma unroll
  for (int d = 0; d < 4; ++d) st.o[d] = f32x16{};
  st.m = -1e30f; st.l = 0.f;

  f32x16 sc = {};
#pragma unroll
  for (int i = 0; i < 8; ++i) sc = mfma32(*(const bf16x8*)(lds + c.kaddr[i]), qf[i], sc);
  st.m = rowmax16(sc);
#pragma unroll
  for (int i = 0; i < 16; ++i) { sc[i] -= st.m; st.negm[i] = -st.m; }
  step<0, 8, true, true, true>(c, st, sc, qf, lds, 0);
  step<1, 8, true, true, true>(c, st, sc, qf, lds, 1);
  {
    const char* wsl = (const char*)(wo + (size_t)(32 * (bid >> 1)) * DM + 288 * (bid & 1));
#pragma unroll
    for (int i = 0; i < 3; ++i) {
      const int p = wid + 8 * i;
      const int idx = p * 64 + lane, row = idx / 36, chn = idx - row * 36;
      dma16(131072 + p * 1024, p < 18 ? (unsigned)(row * 1152 + chn * 16) : 0u, wsl);
    }
  }
  step<2, 8, true, true, true>(c, st, sc, qf, lds, 2);
  step<3, 8, true, true, true>(c, st, sc, qf, lds, 3);
#pragma unroll 1
  for (int t = 4; t < 12; t += 4) {
    step<0, 8, true, true, true>(c, st, sc, qf, lds, t);
    step<1, 8, true, true, true>(c, st, sc, qf, lds, t + 1);
    step<2, 8, true, true, true>(c, st, sc, qf, lds, t + 2);
    step<3, 8, true, true, true>(c, st, sc, qf, lds, t + 3);
  }
  {
    const int wk = bid >> 1;
    const int g4 = lane >> 4, q4 = (lane & 15) >> 2, p4 = lane & 3;
    const char* sb = lds + 131072 + (8 * g4 + q4) * 576 + p4 * 8;
#pragma unroll
    for (int i = 0; i < 3; ++i) {
      const int cc = wid + 8 * i;
      if (cc < 18) {
        s16x4 lo = tr_read(sb + cc * 32), hi = tr_read(sb + cc * 32 + 4 * 576);
        bf16x8 af = __builtin_shufflevector(lo, hi, 0, 1, 2, 3, 4, 5, 6, 7);
        *(bf16x8*)(wot + ((size_t)(wk * 36 + (bid & 1) * 18 + cc) << 10) + lane * 16) = af;
      }
    }
  }
  step<0, 8, false, true, true>(c, st, sc, qf, lds, 12);
  step<1, 6, false, false, true>(c, st, sc, qf, lds, 13);
  step<2, 2, false, false, true>(c, st, sc, qf, lds, 14);
  step<3, 0, false, false, false>(c, st, sc, qf, lds, 15);

  float lt = st.l + __shfl_xor(st.l, 32);
  char* xw = lds + wid * 8704;
  char* xp = lds + (wid ^ 4) * 8704;
  {
#pragma unroll
    for (int dd = 0; dd < 2; ++dd) {
      const f32x16 v = kg ? st.o[dd] : st.o[2 + dd];
#pragma unroll
      for (int a = 0; a < 4; ++a) {
        f32x4 w = {v[4 * a], v[4 * a + 1], v[4 * a + 2], v[4 * a + 3]};
        *(f32x4*)(xw + ((dd * 4 + a) * 64 + lane) * 16) = w;
      }
    }
    f32x2 ml = {st.m, lt};
    *(f32x2*)(xw + 8192 + lane * 8) = ml;
  }
  __syncthreads();
  f32x2 pml = *(const f32x2*)(xp + 8192 + lane * 8);
  const float mm = fmaxf(st.m, pml[0]);
  const float fa = __builtin_amdgcn_exp2f(st.m - mm), fb = __builtin_amdgcn_exp2f(pml[0] - mm);
  const float inv = 1.0f / (lt * fa + pml[1] * fb);
  const float wa = fa * inv, wb = fb * inv;
  char* stg = lds + 8 * 8704 + wid * 4608;
#pragma unroll
  for (int dd = 0; dd < 2; ++dd) {
    const f32x16 own = kg ? st.o[2 + dd] : st.o[dd];
#pragma unroll
    for (int a = 0; a < 4; ++a) {
      f32x4 pv = *(const f32x4*)(xp + ((dd * 4 + a) * 64 + lane) * 16);
      float o0 = own[4 * a] * wa + pv[0] * wb, o1 = own[4 * a + 1] * wa + pv[1] * wb;
      float o2 = own[4 * a + 2] * wa + pv[2] * wb, o3 = own[4 * a + 3] * wa + pv[3] * wb;
      u32x2 w = {cvtpk(o0, o1), cvtpk(o2, o3)};
      *(u32x2*)(stg + r * 144 + (32 * dd + 8 * a + 4 * h) * 2) = w;
    }
  }
  asm volatile("s_waitcnt lgkmcnt(0)" ::: "memory");
  __builtin_amdgcn_wave_barrier();
  {
    const int kst = (head * DH + kg * 64) >> 5;
    const int mt0 = (b * SQ + 32 * pb) >> 4;
#pragma unroll
    for (int i = 0; i < 4; ++i) {
      const int mi = i >> 1, ki = i & 1;
      u32x4 w = *(const u32x4*)(stg + (16 * mi + (lane & 15)) * 144 + ki * 64 + (lane >> 4) * 16);
      char* dst = (char*)og + ((size_t)((kst + ki) * 64 + mt0 + mi) << 10) + lane * 16;
      *(u32x4*)dst = w;
    }
  }
}

DI f32x4 mfma16(bf16x8 a, bf16x8 b, f32x4 c) { return __builtin_amdgcn_mfma_f32_16x16x32_bf16(a, b, c, 0, 0, 0); }
constexpr int OP_LDS = 98304;
__global__ __launch_bounds__(512, 2) void oproj_kernel(const char* __restrict__ og, const char* __restrict__ wot, u16* __restrict__ out) {
  extern __shared__ __attribute__((aligned(16))) char lds[];
  const int bid = blockIdx.x;
  const int xc = bid & 7, j = bid >> 3;
  const int mt = (xc & 3) * 4 + (j & 3), nt = (xc >> 2) * 6 + (j >> 2);
  const int tid = threadIdx.x, lane = tid & 63;
  const int wid = __builtin_amdgcn_readfirstlane(tid >> 6);
  const int rot = ((j >> 2) + 3 * (j & 3)) & 7;
  const unsigned voff = lane * 16;
  const char* ob = og + ((size_t)((8 * wid) * 64 + 4 * mt) << 10);
  const char* wb = wot + ((size_t)((8 * wid) * 36 + 3 * nt) << 10);
  bf16x8 fb[4][4], fa[4][3];
  auto issue = [&](int s) {
    const int ks = (s >> 3) * 64 + ((s + rot) & 7);
    const char* po = ob + (size_t)ks * 65536;
    const char* pw = wb + (size_t)ks * 36864;
#pragma unroll
    for (int i = 0; i < 4; ++i) asm volatile("global_load_dwordx4 %0, %1, %2 offset:%3" : "=v"(fb[s & 3][i]) : "v"(voff), "s"(po), "i"(i * 1024) : "memory");
#pragma unroll
    for (int i = 0; i < 3; ++i) asm volatile("global_load_dwordx4 %0, %1, %2 offset:%3" : "=v"(fa[s & 3][i]) : "v"(voff), "s"(pw), "i"(i * 1024) : "memory");
  };
#pragma unroll
  for (int s = 0; s < 4; ++s) issue(s);
  f32x4 acc[4][3];
#pragma unroll
  for (int i = 0; i < 4; ++i)
#pragma unroll
    for (int c = 0; c < 3; ++c) acc[i][c] = f32x4{0.f, 0.f, 0.f, 0.f};
#pragma unroll
  for (int s = 0; s < 16; ++s) {
    const int young = 7 * ((15 - s) < 3 ? (15 - s) : 3);
    asm volatile("s_waitcnt vmcnt(%7)" : "+v"(fb[s & 3][0]), "+v"(fb[s & 3][1]), "+v"(fb[s & 3][2]), "+v"(fb[s & 3][3]), "+v"(fa[s & 3][0]), "+v"(fa[s & 3][1]), "+v"(fa[s & 3][2]) : "i"(young) : "memory");
#pragma unroll
    for (int i = 0; i < 4; ++i)
#pragma unroll
      for (int c = 0; c < 3; ++c) acc[i][c] = mfma16(fa[s & 3][c], fb[s & 3][i], acc[i][c]);
    __builtin_amdgcn_sched_barrier(0);
    if (s + 4 < 16) issue(s + 4);
    __builtin_amdgcn_sched_barrier(0);
  }
  {
    char* xs = lds + wid * 12288 + lane * 16;
#pragma unroll
    for (int i = 0; i < 4; ++i)
#pragma unroll
      for (int c = 0; c < 3; ++c) *(f32x4*)(xs + (i * 3 + c) * 1024) = acc[i][c];
  }
  __syncthreads();
#pragma unroll
  for (int it = 0; it < 2; ++it) {
    const int ci = tid + 512 * it;
    if (ci < 768) {
      f32x4 sum = *(const f32x4*)(lds + ci * 16);
#pragma unroll
      for (int w = 1; w < 8; ++w) sum += *(const f32x4*)(lds + w * 12288 + ci * 16);
      const int tl = ci >> 6, ln = ci & 63, mi = tl / 3, c = tl - mi * 3;
      u32x2 w2 = {cvtpk(sum[0], sum[1]), cvtpk(sum[2], sum[3])};
      *(u32x2*)(out + (size_t)(64 * mt + 16 * mi + (ln & 15)) * DM + 48 * nt + 16 * c + 4 * (ln >> 4)) = w2;
    }
  }
}

extern "C" void kernel_launch(void* const* d_in, const int* in_sizes, int n_in, void* d_out, int out_size, void* d_ws, size_t ws_size,
                              hipStream_t stream) {
  const u16* x = (const u16*)d_in[0];
  const u16* kc = (const u16*)d_in[1];
  const u16* vc = (const u16*)d_in[2];
  const u16* wq = (const u16*)d_in[3];
  const u16* wo = (const u16*)d_in[4];
  u16* out = (u16*)d_out;
  u16* ows = (u16*)((char*)d_ws + (8 << 20));
  char* wot = (char*)d_ws + (24 << 20);
  (void)hipFuncSetAttribute((const void*)attn_kernel, hipFuncAttributeMaxDynamicSharedMemorySize, ATT_LDS);
  attn_kernel<<<256, 512, ATT_LDS, stream>>>(x, wq, kc, vc, ows, wo, wot);
  (void)hipFuncSetAttribute((const void*)oproj_kernel, hipFuncAttributeMaxDynamicSharedMemorySize, OP_LDS);
  oproj_kernel<<<192, 512, OP_LDS, stream>>>((const char*)ows, wot, out);
}
```

```cpp
#include <hip/hip_runtime.h>
#include <hip/hip_bf16.h>
#include <stdint.h>

typedef __attribute__((ext_vector_type(8))) short bf16x8;
typedef __attribute__((ext_vector_type(4))) short s16x4;
typedef __attribute__((ext_vector_type(16))) float f32x16;
typedef __attribute__((ext_vector_type(4))) float f32x4;
typedef __attribute__((ext_vector_type(2))) float f32x2;
typedef __attribute__((ext_vector_type(4))) unsigned u32x4;
typedef __attribute__((ext_vector_type(2))) unsigned u32x2;
typedef __attribute__((ext_vector_type(2))) __bf16 bf16x2_t;
typedef unsigned short u16;
typedef __attribute__((address_space(3))) s16x4* lds_s16x4_p;

#define DI __device__ __forceinline__

constexpr int NB = 16, SQ = 64, DM = 576, HQ = 32, HKV = 16, SK = 1024, DH = 128;
constexpr int MROWS = NB * SQ;
constexpr int QW = HQ * DH;

DI unsigned cvtpk(float lo, float hi) {
  f32x2 v = {lo, hi};
  bf16x2_t b = __builtin_convertvector(v, bf16x2_t);
  return __builtin_bit_cast(unsigned, b);
}
DI s16x4 tr_read(const char* p) {
  return __builtin_amdgcn_ds_read_tr16_b64_v4i16((lds_s16x4_p)(p));
}
DI f32x16 mfma32(bf16x8 a, bf16x8 b, f32x16 c) { return __builtin_amdgcn_mfma_f32_32x32x16_bf16(a, b, c, 0, 0, 0); }

constexpr int ATT_LDS = 163840;
constexpr float SC = 0.08838834764831845f * 1.4426950408889634f;

DI void dma16(unsigned ldsaddr, unsigned voff, const void* sbase) {
  unsigned keep;
  asm volatile("s_mov_b32 %0, m0\n\ts_mov_b32 m0, %1\n\ts_nop 0\n\tglobal_load_lds_dwordx4 %2, %3 nt\n\ts_mov_b32 m0, %0"
               : "=&s"(keep) : "s"(ldsaddr), "v"(voff), "s"(sbase) : "memory");
}

DI void dma16p(unsigned ldsaddr, unsigned voff, const void* sbase) {
  unsigned keep;
  asm volatile("s_mov_b32 %0, m0\n\ts_mov_b32 m0, %1\n\ts_nop 0\n\tglobal_load_lds_dwordx4 %2, %3\n\ts_mov_b32 m0, %0"
               : "=&s"(keep) : "s"(ldsaddr), "v"(voff), "s"(sbase) : "memory");
}

struct AttnState {
  f32x16 o[4];
  f32x16 negm;
  float m, l;
};

template <int N> DI void wait_vm() { asm volatile("s_waitcnt vmcnt(%0)" ::"i"(N) : "memory"); }

struct AttnCtx {
  const char* kbase;
  const char* vbase;
  unsigned koff[2], voff[2];
  unsigned ldsw;
  int rt;
  unsigned kaddr[8];
  unsigned vaddr[8];
};


#define SBAR() __builtin_amdgcn_sched_barrier(0)
constexpr float THR = 8.0f;
template <int SL> DI void issue_k(const AttnCtx& c, int t) {
  const char* kb = c.kbase + (size_t)((t + c.rt) & 15) * 16384;
  unsigned l0 = SL * 16384 + c.ldsw;
  dma16(l0, c.koff[0], kb);
  dma16(l0 + 1024, c.koff[1], kb);
}
template <int SL> DI void issue_v(const AttnCtx& c, int t) {
  const char* vb = c.vbase + (size_t)((t + c.rt) & 15) * 16384;
  unsigned l0 = 65536 + SL * 16384 + c.ldsw;
  dma16(l0, c.voff[0], vb);
  dma16(l0 + 1024, c.voff[1], vb);
}
DI float rowmax16(const f32x16& s) {
  float pm = s[0];
#pragma unroll
  for (int i = 1; i < 16; ++i) pm = fmaxf(pm, s[i]);
  auto rr = __builtin_amdgcn_permlane32_swap(__float_as_uint(pm), __float_as_uint(pm), false, false);
  return fmaxf(__uint_as_float(rr[0]), __uint_as_float(rr[1]));
}
template <int SL, int WAITN, int WAITL, bool NEXT, class F> DI void step(const AttnCtx& c, AttnState& st, f32x16& sc, const bf16x8* qf, const char* lds, bool lastit, bool extra, F act) {
  if (WAITN != WAITL && lastit) wait_vm<WAITL>(); else if (extra) wait_vm<WAITN + 3>(); else wait_vm<WAITN>();
  asm volatile("s_waitcnt lgkmcnt(0)" ::: "memory");
  __builtin_amdgcn_s_barrier();
  SBAR();
  const char* Ks = lds + ((SL + 1) & 3) * 16384;
  const char* Vs = lds + SL * 16384;
  bf16x8 kf[8];
  if (NEXT) {
#pragma unroll
    for (int i = 0; i < 8; ++i) kf[i] = *(const bf16x8*)(Ks + c.kaddr[i]);
  }
  SBAR();
  act();
  SBAR();
  f32x16 sn;
  if (NEXT) {
    asm("v_mfma_f32_32x32x16_bf16 %0, %1, %2, %3" : "=&v"(sn) : "v"(kf[0]), "v"(qf[0]), "v"(st.negm));
#pragma unroll
    for (int i = 1; i < 8; ++i) sn = mfma32(kf[i], qf[i], sn);
  }
  s16x4 vlo[4][2], vhi[4][2];
#pragma unroll
  for (int k = 0; k < 2; ++k)
#pragma unroll
    for (int d = 0; d < 4; ++d) {
      vlo[d][k] = tr_read(Vs + c.vaddr[2 * d] + k * 4096);
      vhi[d][k] = tr_read(Vs + c.vaddr[2 * d + 1] + k * 4096);
    }
  float ps = 0.f;
#pragma unroll
  for (int i = 0; i < 16; ++i) { sc[i] = __builtin_amdgcn_exp2f(sc[i]); ps += sc[i]; }
  st.l += ps;
  bf16x8 pf[2];
#pragma unroll
  for (int k = 0; k < 2; ++k) {
    u32x4 w = {cvtpk(sc[8 * k], sc[8 * k + 1]), cvtpk(sc[8 * k + 2], sc[8 * k + 3]), cvtpk(sc[8 * k + 4], sc[8 * k + 5]), cvtpk(sc[8 * k + 6], sc[8 * k + 7])};
    pf[k] = __builtin_bit_cast(bf16x8, w);
  }
  if (NEXT) {
#pragma unroll
    for (int i = 0; i < 8; ++i) {
      __builtin_amdgcn_sched_group_barrier(0x8, 1, 0);
      __builtin_amdgcn_sched_group_barrier(0x100, 2, 0);
      __builtin_amdgcn_sched_group_barrier(0x2, 5, 0);
    }
  }
  SBAR();
#pragma unroll
  for (int k = 0; k < 2; ++k) {
#pragma unroll
    for (int d = 0; d < 4; ++d) {
      bf16x8 vf = __builtin_shufflevector(vlo[d][k], vhi[d][k], 0, 1, 2, 3, 4, 5, 6, 7);
      st.o[d] = mfma32(vf, pf[k], st.o[d]);
    }
  }
  if (NEXT) {
    const float pm = rowmax16(sn);
    SBAR();
    if (__builtin_expect(__any(pm > THR), 0)) {
      const float dl = fmaxf(pm, 0.f);
      const float alpha = __builtin_amdgcn_exp2f(-dl);
      st.m += dl;
      st.l *= alpha;
#pragma unroll
      for (int i = 0; i < 16; ++i) { sn[i] -= dl; st.negm[i] = -st.m; }
#pragma unroll
      for (int d = 0; d < 4; ++d)
#pragma unroll
        for (int i = 0; i < 16; ++i) st.o[d][i] *= alpha;
    }
    sc = sn;
  }
  SBAR();
}

template <int N> struct IC { static constexpr int value = N; };

__global__ __launch_bounds__(512, 2) void attn_kernel(const u16* __restrict__ x, const u16* __restrict__ wq, const u16* __restrict__ kc,
                                                      const u16* __restrict__ vc, u16* __restrict__ og,
                                                      const u16* __restrict__ wo, char* __restrict__ wot) {
  extern __shared__ __attribute__((aligned(16))) char lds[];
  const int bid = blockIdx.x;
  const int hkv = 2 * (bid & 7) + ((bid >> 3) & 1), b = bid >> 4;
  const int rot = 2 * ((b * 9) >> 4);
  const int tid = threadIdx.x, lane = tid & 63, r = lane & 31, h = lane >> 5;
  const int wid = __builtin_amdgcn_readfirstlane(tid >> 6);
  const int kg = wid >> 2, g = (wid >> 1) & 1, pb = wid & 1;
  const int head = hkv * 2 + g;
  const int wk = ((bid >> 3) << 2) | (bid & 3), wh = (bid >> 2) & 1;

  const char* wqb = (const char*)(wq + (size_t)hkv * 256);
  const char* xb = (const char*)(x + (size_t)b * SQ * DM);
  constexpr unsigned XB0 = 131072;
  unsigned wsrc[2], xsrc;
#pragma unroll
  for (int i = 0; i < 2; ++i) {
    const int row = 4 * wid + 2 * i + (lane >> 5), pos = lane & 31;
    const int f = ((row & 3) << 2) | ((row >> 2) & 3);
    wsrc[i] = row * 8192 + ((pos ^ f) << 4);
  }
  {
    const int row = 8 * wid + (lane >> 3), pos = lane & 7;
    xsrc = row * 1152 + ((pos ^ ((row >> 1) & 7)) << 4);
  }
  auto issue_w = [&](int chi, unsigned wbuf) {
    int ch = chi + rot; if (ch >= 18) ch -= 18;
    const char* wsb = wqb + (size_t)ch * 32 * 8192;
#pragma unroll
    for (int i = 0; i < 2; ++i) dma16p(wbuf + wid * 2048 + i * 1024, wsrc[i], wsb);
  };
  auto issue_x = [&](int p) {
    int ch = 2 * p + rot; if (ch >= 18) ch -= 18;
    dma16p(XB0 + (p & 3) * 8192 + wid * 1024, xsrc, xb + ch * 64);
  };
  issue_w(0, 98304);
  issue_x(0);
  issue_w(1, 114688);
  issue_w(2, 0);
  issue_w(3, 16384);
  issue_x(1);
  SBAR();
  AttnCtx c;
  c.kbase = (const char*)(kc + (size_t)(b * HKV + hkv) * SK * DH);
  c.vbase = (const char*)(vc + (size_t)(b * HKV + hkv) * SK * DH);
  c.ldsw = wid * 8 * 256;
  c.rt = hkv;
#pragma unroll
  for (int i = 0; i < 2; ++i) {
    const int row = 8 * wid + 4 * i + (lane >> 4), pos = lane & 15;
    c.koff[i] = row * 256 + ((pos ^ (row & 15)) << 4);
    c.voff[i] = row * 256 + ((pos ^ (((row & 3) << 2) | ((row >> 2) & 3))) << 4);
  }
  {
    const int krow = 32 * kg + r;
#pragma unroll
    for (int i = 0; i < 8; ++i) c.kaddr[i] = krow * 256 + (((2 * i + h) ^ (r & 15)) << 4);
    const int blk = (lane >> 4) & 1, q4 = (lane & 15) >> 2, p4 = lane & 3;
#pragma unroll
    for (int d = 0; d < 4; ++d)
#pragma unroll
      for (int t = 0; t < 2; ++t) {
        const int row = 32 * kg + 8 * t + 4 * h + q4;
        const int ch = 4 * (d ^ (2 * kg)) + 2 * blk + (p4 >> 1);
        const int sw = ((row & 3) << 2) | ((row >> 2) & 3);
        c.vaddr[2 * d + t] = 65536 + row * 256 + ((ch ^ sw) << 4) + 8 * (p4 & 1);
        asm volatile("" : "+v"(c.vaddr[2 * d + t]));
      }
  }
  unsigned waddr[2][2], xaddr[4];
  {
    const int blk = (lane >> 4) & 1, q4 = (lane & 15) >> 2, p4 = lane & 3;
#pragma unroll
    for (int j = 0; j < 2; ++j)
#pragma unroll
      for (int t = 0; t < 2; ++t) {
        const int row = 8 * h + 4 * t + q4;
        const int chn = 4 * (4 * g + 2 * kg + j) + 2 * blk + (p4 >> 1);
        const int f = ((row & 3) << 2) | ((row >> 2) & 3);
        waddr[j][t] = row * 512 + ((chn ^ f) << 4) + 8 * (p4 & 1);
      }
    const int row = 32 * pb + r;
#pragma unroll
    for (int s = 0; s < 4; ++s) xaddr[s] = row * 128 + (((2 * s + h) ^ ((row >> 1) & 7)) << 4);
  }
  f32x16 qa[2] = {};
  bf16x8 qf[8];
  AttnState st;
  f32x16 sc;
  auto chunk = [&](auto WN, unsigned wbuf, unsigned xbuf, int e, auto act) {
    wait_vm<decltype(WN)::value>();
    asm volatile("s_waitcnt lgkmcnt(0)" ::: "memory");
    __builtin_amdgcn_s_barrier();
    bf16x8 xf[2];
    s16x4 lo[2][2], hi[2][2];
#pragma unroll
    for (int s = 0; s < 2; ++s) {
      xf[s] = *(const bf16x8*)(lds + xbuf + xaddr[2 * e + s]);
#pragma unroll
      for (int j = 0; j < 2; ++j) {
        lo[s][j] = tr_read(lds + wbuf + s * 8192 + waddr[j][0]);
        hi[s][j] = tr_read(lds + wbuf + s * 8192 + waddr[j][1]);
      }
    }
    SBAR();
    act();
    SBAR();
#pragma unroll
    for (int s = 0; s < 2; ++s)
#pragma unroll
      for (int j = 0; j < 2; ++j)
        qa[j] = mfma32(__builtin_shufflevector(lo[s][j], hi[s][j], 0, 1, 2, 3, 4, 5, 6, 7), xf[s], qa[j]);
  };
  {
  chunk(IC<7>{}, 98304, XB0 + 0, 0, [&]() { issue_w(4, 65536); issue_w(5, 32768); issue_x(2); });
  chunk(IC<10>{}, 114688, XB0 + 0, 1, [&]() { issue_w(6, 81920); issue_w(7, 49152); issue_x(3); });
  chunk(IC<10>{}, 0, XB0 + 8192, 0, [&]() { issue_w(8, 98304); issue_w(9, 114688); issue_x(4); });
  chunk(IC<15>{}, 16384, XB0 + 8192, 1, [&]() { issue_w(10, 0); });
  chunk(IC<12>{}, 65536, XB0 + 16384, 0, [&]() { issue_x(5); issue_w(11, 16384); });
  chunk(IC<15>{}, 32768, XB0 + 16384, 1, [&]() { issue_w(12, 65536); });
  chunk(IC<12>{}, 81920, XB0 + 24576, 0, [&]() { issue_x(6); issue_w(13, 32768); });
  chunk(IC<15>{}, 49152, XB0 + 24576, 1, [&]() { issue_w(14, 81920); });
  chunk(IC<12>{}, 98304, XB0 + 0, 0, [&]() { issue_x(7); issue_w(15, 49152); });
  chunk(IC<15>{}, 114688, XB0 + 0, 1, [&]() { issue_w(16, 98304); });
  chunk(IC<14>{}, 0, XB0 + 8192, 0, [&]() { issue_x(8); issue_w(17, 114688); });
  chunk(IC<15>{}, 16384, XB0 + 8192, 1, [&]() { issue_k<0>(c, 0); });
  chunk(IC<14>{}, 65536, XB0 + 16384, 0, [&]() { issue_k<1>(c, 1); });
  chunk(IC<14>{}, 32768, XB0 + 16384, 1, [&]() { issue_v<0>(c, 0); });
  chunk(IC<13>{}, 81920, XB0 + 24576, 0, [&]() { issue_k<2>(c, 2); });
  chunk(IC<13>{}, 49152, XB0 + 24576, 1, [&]() {  });
  chunk(IC<10>{}, 98304, XB0 + 0, 0, [&]() { issue_v<1>(c, 1); });
  chunk(IC<10>{}, 114688, XB0 + 0, 1, [&]() {  });
  asm volatile("s_waitcnt lgkmcnt(0)" ::: "memory");
  __builtin_amdgcn_s_barrier();
  {
    char* qt = lds + 131072 + (g * 2 + pb) * 8192 + r * 256;
#pragma unroll
    for (int j = 0; j < 2; ++j)
#pragma unroll
      for (int a = 0; a < 4; ++a) {
        unsigned w0 = cvtpk(qa[j][4 * a], qa[j][4 * a + 1]), w1 = cvtpk(qa[j][4 * a + 2], qa[j][4 * a + 3]);
        w0 = cvtpk(__uint_as_float(w0 << 16) * SC, __uint_as_float(w0 & 0xffff0000u) * SC);
        w1 = cvtpk(__uint_as_float(w1 << 16) * SC, __uint_as_float(w1 & 0xffff0000u) * SC);
        u32x2 w = {w0, w1};
        *(u32x2*)(qt + (((8 * kg + 4 * j + a) ^ (r & 15)) << 4) + 8 * h) = w;
      }
  }
  issue_k<3>(c, 3);
  wait_vm<10>();
  asm volatile("s_waitcnt lgkmcnt(0)" ::: "memory");
  __builtin_amdgcn_s_barrier();
  {
    const char* qt = lds + 131072 + (g * 2 + pb) * 8192 + r * 256;
#pragma unroll
    for (int i = 0; i < 8; ++i) qf[i] = *(const bf16x8*)(qt + (((2 * i + h) ^ (r & 15)) << 4));
  }

#pragma unroll
  for (int d = 0; d < 4; ++d) st.o[d] = f32x16{};
  st.m = -1e30f; st.l = 0.f;

  issue_v<2>(c, 2);
  sc = f32x16{};
#pragma unroll
  for (int i = 0; i < 8; ++i) sc = mfma32(*(const bf16x8*)(lds + c.kaddr[i]), qf[i], sc);
  st.m = rowmax16(sc);
#pragma unroll
  for (int i = 0; i < 16; ++i) { sc[i] -= st.m; st.negm[i] = -st.m; }
  }
#pragma unroll 1
  for (int t = 0; t < 16; t += 4) {
    asm volatile("" : "+s"(t));
    const bool lastit = t == 12;
    step<0, 8, 8, true>(c, st, sc, qf, lds, lastit, t == 4, [&]() { if (!lastit) issue_k<0>(c, t + 4); issue_v<3>(c, t + 3); });
    step<1, 8, 6, true>(c, st, sc, qf, lds, lastit, false, [&]() {
      if (!lastit) { issue_k<1>(c, t + 5); issue_v<0>(c, t + 4); }
      if (t == 0) {
        const char* wsl = (const char*)(wo + (size_t)(32 * wk) * DM + 288 * wh);
#pragma unroll
        for (int i = 0; i < 3; ++i) {
          const int p = wid + 8 * i;
          const int idx = p * 64 + lane, row = idx / 36, chn = idx - row * 36;
          dma16(131072 + p * 1024, p < 18 ? (unsigned)(row * 1152 + chn * 16) : 0u, wsl);
        }
      }
    });
    step<2, 8, 2, true>(c, st, sc, qf, lds, lastit, t == 0, [&]() { if (!lastit) { issue_k<2>(c, t + 6); issue_v<1>(c, t + 5); } });
    if (lastit) break;
    step<3, 8, 8, true>(c, st, sc, qf, lds, false, t == 0, [&]() { issue_k<3>(c, t + 7); issue_v<2>(c, t + 6); });
  }
  {
    float ps = 0.f;
#pragma unroll
    for (int i = 0; i < 16; ++i) { sc[i] = __builtin_amdgcn_exp2f(sc[i]); ps += sc[i]; }
    st.l += ps;
    bf16x8 pf[2];
#pragma unroll
    for (int k = 0; k < 2; ++k) {
      u32x4 w = {cvtpk(sc[8 * k], sc[8 * k + 1]), cvtpk(sc[8 * k + 2], sc[8 * k + 3]), cvtpk(sc[8 * k + 4], sc[8 * k + 5]), cvtpk(sc[8 * k + 6], sc[8 * k + 7])};
      pf[k] = __builtin_bit_cast(bf16x8, w);
    }
    st.l += __shfl_xor(st.l, 32);
    {
      f32x2 ml = {st.m, st.l};
      *(f32x2*)(lds + 155648 + wid * 512 + lane * 8) = ml;
    }
    SBAR();
    wait_vm<0>();
    asm volatile("s_waitcnt lgkmcnt(0)" ::: "memory");
    __builtin_amdgcn_s_barrier();
    SBAR();
    const char* Vs = lds + 3 * 16384;
#pragma unroll
    for (int e = 0; e < 4; ++e) {
      const int d = e ^ 2;
#pragma unroll
      for (int k = 0; k < 2; ++k) {
        s16x4 lo = tr_read(Vs + c.vaddr[2 * d] + k * 4096), hi = tr_read(Vs + c.vaddr[2 * d + 1] + k * 4096);
        st.o[d] = mfma32(__builtin_shufflevector(lo, hi, 0, 1, 2, 3, 4, 5, 6, 7), pf[k], st.o[d]);
      }
    }
  }
  {
    const int g4 = lane >> 4, q4 = (lane & 15) >> 2, p4 = lane & 3;
    const char* sb = lds + 131072 + (8 * g4 + q4) * 576 + p4 * 8;
#pragma unroll
    for (int i = 0; i < 3; ++i) {
      const int cc = wid + 8 * i;
      if (cc < 18) {
        s16x4 lo = tr_read(sb + cc * 32), hi = tr_read(sb + cc * 32 + 4 * 576);
        bf16x8 af = __builtin_shufflevector(lo, hi, 0, 1, 2, 3, 4, 5, 6, 7);
        asm volatile("global_store_dwordx4 %0, %1, off sc1\n\ts_nop 1" :: "v"(wot + ((size_t)(wk * 36 + wh * 18 + cc) << 10) + lane * 16), "v"(af) : "memory");
      }
    }
  }

  const float lt = st.l;
  f32x2 pml = *(const f32x2*)(lds + 155648 + (wid ^ 4) * 512 + lane * 8);
  const float mm = fmaxf(st.m, pml[0]);
  const float fa = __builtin_amdgcn_exp2f(st.m - mm), fb = __builtin_amdgcn_exp2f(pml[0] - mm);
  const float inv = 1.0f / (lt * fa + pml[1] * fb);
  const float wa = fa * inv, wb = fb * inv;
  char* xw = lds + wid * 8704;
  char* xp = lds + (wid ^ 4) * 8704;
  {
#pragma unroll
    for (int dd = 0; dd < 2; ++dd) {
      const f32x16 v = st.o[2 + dd];
#pragma unroll
      for (int a = 0; a < 4; ++a) {
        f32x4 w = {v[4 * a], v[4 * a + 1], v[4 * a + 2], v[4 * a + 3]};
        *(f32x4*)(xw + ((dd * 4 + a) * 64 + lane) * 16) = w;
      }
    }
  }
  __syncthreads();
  const int kst = (head * DH + kg * 64) >> 5;
  const int mt0 = (b * SQ + 32 * pb) >> 4;
  char* dst0 = (char*)og + ((size_t)(kst * 64 + mt0 + (r >> 4)) << 10) + (h * 16 + (r & 15)) * 16;
#pragma unroll
  for (int dd = 0; dd < 2; ++dd) {
    const f32x16 own = st.o[dd];
    unsigned w[4][2];
#pragma unroll
    for (int a = 0; a < 4; ++a) {
      f32x4 pv = *(const f32x4*)(xp + ((dd * 4 + a) * 64 + lane) * 16);
      float o0 = own[4 * a] * wa + pv[0] * wb, o1 = own[4 * a + 1] * wa + pv[1] * wb;
      float o2 = own[4 * a + 2] * wa + pv[2] * wb, o3 = own[4 * a + 3] * wa + pv[3] * wb;
      w[a][0] = cvtpk(o0, o1); w[a][1] = cvtpk(o2, o3);
    }
#pragma unroll
    for (int i = 0; i < 2; ++i) {
      auto s0 = __builtin_amdgcn_permlane32_swap(w[2 * i][0], w[2 * i + 1][0], false, false);
      auto s1 = __builtin_amdgcn_permlane32_swap(w[2 * i][1], w[2 * i + 1][1], false, false);
      u32x4 v = {s0[0], s1[0], s0[1], s1[1]};
      char* dst = dst0 + dd * 65536 + i * 512;
      asm volatile("global_store_dwordx4 %0, %1, off sc1\n\ts_nop 1" :: "v"(dst), "v"(v) : "memory");
    }
  }
}

DI f32x4 mfma16(bf16x8 a, bf16x8 b, f32x4 c) { return __builtin_amdgcn_mfma_f32_16x16x32_bf16(a, b, c, 0, 0, 0); }
constexpr int OP_LDS = 98304;
constexpr int KD = 2;
__global__ __launch_bounds__(512, 2) void oproj_kernel(const char* __restrict__ og, const char* __restrict__ wot, u16* __restrict__ out) {
  extern __shared__ __attribute__((aligned(16))) char lds[];
  const int bid = blockIdx.x;
  const int xc = bid & 7, j = bid >> 3;
  const int mt = (xc & 3) * 4 + (j & 3), nt = (xc >> 2) * 6 + (j >> 2);
  const int tid = threadIdx.x, lane = tid & 63;
  const int wid = __builtin_amdgcn_readfirstlane(tid >> 6);
  const int rot = (2 * (j >> 2) + ((j >> 3) & 1) + (j & 3)) & 3;
  const unsigned voff = lane * 16;
  const char* ob = og + ((size_t)((4 * wid) * 64 + 4 * mt) << 10);
  const char* wb = wot + ((size_t)((4 * wid) * 36 + 3 * nt) << 10);
  bf16x8 fb[KD][4], fa[KD][3];
  auto issue = [&](int s) {
    const int ks = (s >> 2) * 32 + ((s + rot) & 3);
    const char* po = ob + (size_t)ks * 65536;
    const char* pw = wb + (size_t)ks * 36864;
#pragma unroll
    for (int i = 0; i < 4; ++i) asm volatile("global_load_dwordx4 %0, %1, %2 offset:%3" : "=v"(fb[s % KD][i]) : "v"(voff), "s"(po), "i"(i * 1024) : "memory");
#pragma unroll
    for (int i = 0; i < 3; ++i) asm volatile("global_load_dwordx4 %0, %1, %2 offset:%3" : "=v"(fa[s % KD][i]) : "v"(voff), "s"(pw), "i"(i * 1024) : "memory");
  };
#pragma unroll
  for (int s = 0; s < KD; ++s) issue(s);
  f32x4 acc[4][3];
#pragma unroll
  for (int i = 0; i < 4; ++i)
#pragma unroll
    for (int c = 0; c < 3; ++c) acc[i][c] = f32x4{0.f, 0.f, 0.f, 0.f};
#pragma unroll
  for (int s = 0; s < 16; ++s) {
    const int young = 7 * ((15 - s) < (KD - 1) ? (15 - s) : (KD - 1));
    asm volatile("s_waitcnt vmcnt(%7)" : "+v"(fb[s % KD][0]), "+v"(fb[s % KD][1]), "+v"(fb[s % KD][2]), "+v"(fb[s % KD][3]), "+v"(fa[s % KD][0]), "+v"(fa[s % KD][1]), "+v"(fa[s % KD][2]) : "i"(young) : "memory");
#pragma unroll
    for (int i = 0; i < 4; ++i)
#pragma unroll
      for (int c = 0; c < 3; ++c) acc[i][c] = mfma16(fa[s % KD][c], fb[s % KD][i], acc[i][c]);
    __builtin_amdgcn_sched_barrier(0);
    if (s + KD < 16) issue(s + KD);
    __builtin_amdgcn_sched_barrier(0);
  }
  {
    char* xs = lds + wid * 12288 + lane * 16;
#pragma unroll
    for (int i = 0; i < 4; ++i)
#pragma unroll
      for (int c = 0; c < 3; ++c) *(f32x4*)(xs + (i * 3 + c) * 1024) = acc[i][c];
  }
  __syncthreads();
#pragma unroll
  for (int it = 0; it < 2; ++it) {
    const int ci = tid + 512 * it;
    if (ci < 768) {
      f32x4 sum = *(const f32x4*)(lds + ci * 16);
#pragma unroll
      for (int w = 1; w < 8; ++w) sum += *(const f32x4*)(lds + w * 12288 + ci * 16);
      const int tl = ci >> 6, ln = ci & 63, mi = tl / 3, c = tl - mi * 3;
      u32x2 w2 = {cvtpk(sum[0], sum[1]), cvtpk(sum[2], sum[3])};
      asm volatile("global_store_dwordx2 %0, %1, off sc1\n\ts_nop 1" :: "v"(out + (size_t)(64 * mt + 16 * mi + (ln & 15)) * DM + 48 * nt + 16 * c + 4 * (ln >> 4)), "v"(w2) : "memory");
    }
  }
}

extern "C" void kernel_launch(void* const* d_in, const int* in_sizes, int n_in, void* d_out, int out_size, void* d_ws, size_t ws_size,
                              hipStream_t stream) {
  const u16* x = (const u16*)d_in[0];
  const u16* kc = (const u16*)d_in[1];
  const u16* vc = (const u16*)d_in[2];
  const u16* wq = (const u16*)d_in[3];
  const u16* wo = (const u16*)d_in[4];
  u16* out = (u16*)d_out;
  u16* ows = (u16*)((char*)d_ws + (8 << 20));
  char* wot = (char*)d_ws + (24 << 20);
  (void)hipFuncSetAttribute((const void*)attn_kernel, hipFuncAttributeMaxDynamicSharedMemorySize, ATT_LDS);
  attn_kernel<<<256, 512, ATT_LDS, stream>>>(x, wq, kc, vc, ows, wo, wot);
  (void)hipFuncSetAttribute((const void*)oproj_kernel, hipFuncAttributeMaxDynamicSharedMemorySize, OP_LDS);
  oproj_kernel<<<192, 512, OP_LDS, stream>>>((const char*)ows, wot, out);
}
```
